```python
import torch

def _softmax(x, axis=-1):
    return torch.softmax(x, dim=axis)
def _gelu(x):
    return torch.nn.functional.gelu(x, approximate="tanh")
def _silu(x):
    return torch.nn.functional.silu(x)
def _sigmoid(x):
    return torch.sigmoid(x)
def _layernorm(x, axis=-1, eps=1e-5):
    m = x.mean(dim=axis, keepdim=True)
    v = x.var(dim=axis, keepdim=True, unbiased=False)
    return (x - m) / torch.sqrt(v + eps)
def _scatter_add(tgt, idx, src, axis=0):
    return tgt.clone().index_add_(axis, idx, src)
def _segment_sum(src, seg, n):
    out = torch.zeros((n,) + src.shape[1:], dtype=src.dtype)
    return out.index_add_(0, seg, src)
def _pad(x, pw):
    flat = tuple(v for pair in reversed(pw) for v in pair)
    return torch.nn.functional.pad(x, flat)

kernel_name = 'gated_mix__spectral_fnet__mha_block__prompt'

def setup_inputs():

    _w = (lambda a: (lambda t: t.to(torch.bfloat16) if t.is_floating_point() else t)(torch.as_tensor(a)))
    def _A_setup():
        B, S, D = 1, 2048, 1024
        ns = torch.arange(S)[:, None] * torch.arange(S)[None, :]
        nd = torch.arange(D)[:, None] * torch.arange(D)[None, :]
        Ws_re = torch.cos(-2.0 * torch.pi * ns / S).to(float)
        Ws_im = torch.sin(-2.0 * torch.pi * ns / S).to(float)
        Wd_re = torch.cos(-2.0 * torch.pi * nd / D).to(float)
        Wd_im = torch.sin(-2.0 * torch.pi * nd / D).to(float)
        return {
            "x": _w((lambda *s: torch.randn(*s, dtype=torch.bfloat16) * 0.05)(B, S, D)),
            "dft_seq_re": _w(Ws_re),
            "dft_seq_im": _w(Ws_im),
            "dft_hid_re": _w(Wd_re),
            "dft_hid_im": _w(Wd_im),
        }
    _A_in = _A_setup()

    def _B_setup():
        B, S, D, H = 1, 2048, 1024, 32
        return {
            "x": _w((lambda *s: torch.randn(*s, dtype=torch.bfloat16) * 0.05)(B, S, D)),
            "wqkv": _w((lambda *s: torch.randn(*s, dtype=torch.bfloat16) * 1.6)(D, 3 * D)),
            "wo": _w((lambda *s: torch.randn(*s, dtype=torch.bfloat16) * 0.2)(D, D)),
        }
    _B_in = _B_setup()

    return {
        "A_x": _A_in["x"],
        "A_dft_seq_re": _A_in["dft_seq_re"],
        "A_dft_seq_im": _A_in["dft_seq_im"],
        "A_dft_hid_re": _A_in["dft_hid_re"],
        "A_dft_hid_im": _A_in["dft_hid_im"],
        "B_x": _B_in["x"],
        "B_wqkv": _B_in["wqkv"],
        "B_wo": _B_in["wo"],
        "gate_w": _w((lambda *s: torch.randn(*s, dtype=torch.bfloat16) * 0.05)(1024) * 16.0),
    }

def reference(A_x, A_dft_seq_re, A_dft_seq_im, A_dft_hid_re, A_dft_hid_im, B_x, B_wqkv, B_wo, gate_w):
    def _A_ref(x, dft_seq_re, dft_seq_im, dft_hid_re, dft_hid_im):
        a = torch.einsum("bsd,ed->bse", x, dft_hid_re)
        b = torch.einsum("bsd,ed->bse", x, dft_hid_im)
        y = torch.einsum("ts,bse->bte", dft_seq_re, a) - torch.einsum("ts,bse->bte", dft_seq_im, b)
        return y

    def _B_ref(x, wqkv, wo):
        B, S, D = x.shape
        H = 32
        Dh = D // H
        qkv = (lambda x, *d: x.permute(*d))((x @ wqkv).reshape(B, S, 3, H, Dh), 2, 0, 3, 1, 4)
        q, k, v = qkv[0], qkv[1], qkv[2]
        scores = torch.einsum("bhqd,bhkd->bhqk", q, k) * (Dh ** -0.5)
        p = _softmax(scores, axis=-1)
        o = torch.einsum("bhqk,bhkd->bhqd", p, v)
        return (lambda x, *d: x.permute(*d))(o, 0, 2, 1, 3).reshape(B, S, D) @ wo

    x = A_x
    g = _sigmoid((x * gate_w).sum(-1, keepdims=True))
    a = _A_ref(x, A_dft_seq_re, A_dft_seq_im, A_dft_hid_re, A_dft_hid_im)
    b = _B_ref(x, B_wqkv, B_wo)
    return g * a + (1 - g) * b
```

```cpp
#include <hip/hip_runtime.h>
#include <hip/hip_bf16.h>
#include <stdint.h>
typedef unsigned short u16;
using bf16x8 = __attribute__((ext_vector_type(8))) short;
using f32x16 = __attribute__((ext_vector_type(16))) float;
__device__ __forceinline__ float bf2f(u16 b){ return __uint_as_float(((unsigned)b)<<16); }
__device__ __forceinline__ u16 f2bf(float f){ __hip_bfloat16 h = __float2bfloat16(f); return *(u16*)&h; }
constexpr int S=2048, D=1024, H=32, DH=32;

__global__ void gate_k(const u16* x, const u16* gw, float* g){
  int row = blockIdx.x; int lane = threadIdx.x;
  float s=0;
  for(int i=lane;i<D;i+=64){ s += bf2f(f2bf(bf2f(x[row*D+i])*bf2f(gw[i]))); }
  for(int o=32;o>0;o>>=1) s += __shfl_xor(s,o);
  s = bf2f(f2bf(s));
  if(lane==0) g[row] = bf2f(f2bf(1.f/(1.f+__expf(-s))));
}
template<bool BT>
__global__ void gemm_k(const u16* A, int lda, const u16* B, int ldb, u16* C, int ldc, int K, const float* rs, float mul){
  int lane = threadIdx.x & 63; int w = threadIdx.x>>6;
  int m0 = blockIdx.y*64 + (w>>1)*32, n0 = blockIdx.x*64 + (w&1)*32;
  int r = lane&31, h = lane>>5;
  f32x16 acc = {};
  for(int k=0;k<K;k+=16){
    bf16x8 a = *(const bf16x8*)(A + (size_t)(m0+r)*lda + k + 8*h);
    bf16x8 b;
    if(BT) b = *(const bf16x8*)(B + (size_t)(n0+r)*ldb + k + 8*h);
    else { for(int j=0;j<8;j++) b[j] = (short)B[(size_t)(k+8*h+j)*ldb + n0 + r]; }
    acc = __builtin_amdgcn_mfma_f32_32x32x16_bf16(a,b,acc,0,0,0);
  }
  for(int i=0;i<16;i++){ int row = m0 + (i&3)+8*(i>>2)+4*h; float v = acc[i]*mul; if(rs) v*=rs[row&(S-1)]; C[(size_t)row*ldc + n0 + r] = f2bf(v); }
}
__global__ void final_k(const u16* P, const u16* Q, const u16* O, const u16* Wre, const u16* Wim, const u16* wo, u16* out){
  int lane = threadIdx.x & 63; int w = threadIdx.x>>6;
  int m0 = blockIdx.y*64 + (w>>1)*32, n0 = blockIdx.x*64 + (w&1)*32;
  int r = lane&31, h = lane>>5;
  f32x16 acc = {};
  for(int k=0;k<D;k+=16){
    bf16x8 a = *(const bf16x8*)(P + (size_t)(m0+r)*D + k + 8*h);
    bf16x8 b = *(const bf16x8*)(Wre + (size_t)(n0+r)*D + k + 8*h);
    acc = __builtin_amdgcn_mfma_f32_32x32x16_bf16(a,b,acc,0,0,0);
    a = *(const bf16x8*)(Q + (size_t)(m0+r)*D + k + 8*h);
    b = *(const bf16x8*)(Wim + (size_t)(n0+r)*D + k + 8*h);
    acc = __builtin_amdgcn_mfma_f32_32x32x16_bf16(a,b,acc,0,0,0);
    a = *(const bf16x8*)(O + (size_t)(m0+r)*D + k + 8*h);
    for(int j=0;j<8;j++) b[j] = (short)wo[(size_t)(k+8*h+j)*D + n0 + r];
    acc = __builtin_amdgcn_mfma_f32_32x32x16_bf16(a,b,acc,0,0,0);
  }
  for(int i=0;i<16;i++){ int row = m0 + (i&3)+8*(i>>2)+4*h; out[(size_t)row*D + n0 + r] = f2bf(acc[i]); }
}
__global__ void attn_k(const u16* qkv, const float* g, u16* O){
  __shared__ float ks[64][33], vs[64][33];
  int h = blockIdx.y; int q = blockIdx.x*256 + threadIdx.x;
  float qr[32], o[32];
  #pragma unroll
  for(int d=0;d<32;d++){ qr[d] = bf2f(qkv[(size_t)q*3072 + h*32 + d]) * 0.17677669529f; o[d]=0; }
  float m=-1e30f, l=0;
  for(int k0=0;k0<S;k0+=64){
    __syncthreads();
    for(int i=threadIdx.x;i<64*32;i+=256){ int kk=i>>5,d=i&31; ks[kk][d]=bf2f(qkv[(size_t)(k0+kk)*3072+1024+h*32+d]); vs[kk][d]=bf2f(qkv[(size_t)(k0+kk)*3072+2048+h*32+d]); }
    __syncthreads();
    for(int kk=0;kk<64;kk++){
      float s=0;
      #pragma unroll
      for(int d=0;d<32;d++) s+=qr[d]*ks[kk][d];
      float mn=fmaxf(m,s); float a=__expf(m-mn), p=__expf(s-mn); l=l*a+p; m=mn;
      #pragma unroll
      for(int d=0;d<32;d++) o[d]=o[d]*a+p*vs[kk][d];
    }
  }
  float sc=(1.f-g[q])/l;
  #pragma unroll
  for(int d=0;d<32;d++) O[(size_t)q*D+h*32+d]=f2bf(o[d]*sc);
}
extern "C" void kernel_launch(void* const* d_in, const int* in_sizes, int n_in, void* d_out, int out_size, void* d_ws, size_t ws_size, hipStream_t stream){
  const u16* x=(const u16*)d_in[0]; const u16* sre=(const u16*)d_in[1]; const u16* sim=(const u16*)d_in[2];
  const u16* dre=(const u16*)d_in[3]; const u16* dim_=(const u16*)d_in[4]; const u16* wqkv=(const u16*)d_in[6]; const u16* wo=(const u16*)d_in[7]; const u16* gw=(const u16*)d_in[8];
  char* ws=(char*)d_ws;
  float* g=(float*)ws; u16* qkv=(u16*)(ws+(1<<20)); u16* P=(u16*)(ws+(16<<20)); u16* Q=(u16*)(ws+(24<<20)); u16* O=(u16*)(ws+(32<<20));
  gate_k<<<S,64,0,stream>>>(x,gw,g);
  gemm_k<false><<<dim3(3072/64,S/64),256,0,stream>>>(x,D,wqkv,3072,qkv,3072,D,nullptr,1.f);
  gemm_k<false><<<dim3(D/64,S/64),256,0,stream>>>(sre,S,x,D,P,D,S,g,1.f);
  gemm_k<false><<<dim3(D/64,S/64),256,0,stream>>>(sim,S,x,D,Q,D,S,g,-1.f);
  attn_k<<<dim3(S/256,H),256,0,stream>>>(qkv,g,O);
  final_k<<<dim3(D/64,S/64),256,0,stream>>>(P,Q,O,dre,dim_,wo,(u16*)d_out);
}
```

```cpp
#define MULT 0
#define AUX 0
#define WHICH 0
#include <hip/hip_runtime.h>
#include <hip/hip_bf16.h>
#include <stdint.h>
typedef unsigned short u16;
using bf16x8 = __attribute__((ext_vector_type(8))) short;
using f32x16 = __attribute__((ext_vector_type(16))) float;
__device__ __forceinline__ float bf2f(u16 b){ return __uint_as_float(((unsigned)b)<<16); }
__device__ __forceinline__ u16 f2bf(float f){ __hip_bfloat16 h = __float2bfloat16(f); return *(u16*)&h; }
constexpr int S=2048, D=1024, H=32, DH=32;

__global__ void gate_k(const u16* x, const u16* gw, float* g){
  int row = blockIdx.x; int lane = threadIdx.x;
  float s=0;
  for(int i=lane;i<D;i+=64){ s += bf2f(f2bf(bf2f(x[row*D+i])*bf2f(gw[i]))); }
  for(int o=32;o>0;o>>=1) s += __shfl_xor(s,o);
  s = bf2f(f2bf(s));
  if(lane==0) g[row] = bf2f(f2bf(1.f/(1.f+__expf(-s))));
}
template<bool BT>
__global__ void gemm_k(const u16* A, int lda, const u16* B, int ldb, u16* C, int ldc, int K, const float* rs, float mul){
  int lane = threadIdx.x & 63; int w = threadIdx.x>>6;
  int m0 = blockIdx.y*64 + (w>>1)*32, n0 = blockIdx.x*64 + (w&1)*32;
  int r = lane&31, h = lane>>5;
  f32x16 acc = {};
  for(int k=0;k<K;k+=16){
    bf16x8 a = *(const bf16x8*)(A + (size_t)(m0+r)*lda + k + 8*h);
    bf16x8 b;
    if(BT) b = *(const bf16x8*)(B + (size_t)(n0+r)*ldb + k + 8*h);
    else { for(int j=0;j<8;j++) b[j] = (short)B[(size_t)(k+8*h+j)*ldb + n0 + r]; }
    acc = __builtin_amdgcn_mfma_f32_32x32x16_bf16(a,b,acc,0,0,0);
  }
  for(int i=0;i<16;i++){ int row = m0 + (i&3)+8*(i>>2)+4*h; float v = acc[i]*mul; if(rs) v*=rs[row&(S-1)]; C[(size_t)row*ldc + n0 + r] = f2bf(v); }
}
__global__ void final_k(const u16* P, const u16* Q, const u16* O, const u16* Wre, const u16* Wim, const u16* wo, u16* out){
  int lane = threadIdx.x & 63; int w = threadIdx.x>>6;
  int m0 = blockIdx.y*64 + (w>>1)*32, n0 = blockIdx.x*64 + (w&1)*32;
  int r = lane&31, h = lane>>5;
  f32x16 acc = {};
  for(int k=0;k<D;k+=16){
    bf16x8 a = *(const bf16x8*)(P + (size_t)(m0+r)*D + k + 8*h);
    bf16x8 b = *(const bf16x8*)(Wre + (size_t)(n0+r)*D + k + 8*h);
    acc = __builtin_amdgcn_mfma_f32_32x32x16_bf16(a,b,acc,0,0,0);
    a = *(const bf16x8*)(Q + (size_t)(m0+r)*D + k + 8*h);
    b = *(const bf16x8*)(Wim + (size_t)(n0+r)*D + k + 8*h);
    acc = __builtin_amdgcn_mfma_f32_32x32x16_bf16(a,b,acc,0,0,0);
    a = *(const bf16x8*)(O + (size_t)(m0+r)*D + k + 8*h);
    for(int j=0;j<8;j++) b[j] = (short)wo[(size_t)(k+8*h+j)*D + n0 + r];
    acc = __builtin_amdgcn_mfma_f32_32x32x16_bf16(a,b,acc,0,0,0);
  }
  for(int i=0;i<16;i++){ int row = m0 + (i&3)+8*(i>>2)+4*h; out[(size_t)row*D + n0 + r] = f2bf(acc[i]); }
}
__global__ void attn_k(const u16* qkv, const float* g, u16* O){
  __shared__ float ks[64][33], vs[64][33];
  int h = blockIdx.y; int q = blockIdx.x*256 + threadIdx.x;
  float qr[32], o[32];
  #pragma unroll
  for(int d=0;d<32;d++){ qr[d] = bf2f(qkv[(size_t)q*3072 + h*32 + d]) * 0.17677669529f; o[d]=0; }
  float m=-1e30f, l=0;
  for(int k0=0;k0<S;k0+=64){
    __syncthreads();
    for(int i=threadIdx.x;i<64*32;i+=256){ int kk=i>>5,d=i&31; ks[kk][d]=bf2f(qkv[(size_t)(k0+kk)*3072+1024+h*32+d]); vs[kk][d]=bf2f(qkv[(size_t)(k0+kk)*3072+2048+h*32+d]); }
    __syncthreads();
    for(int kk=0;kk<64;kk++){
      float s=0;
      #pragma unroll
      for(int d=0;d<32;d++) s+=qr[d]*ks[kk][d];
      float mn=fmaxf(m,s); float a=__expf(m-mn), p=__expf(s-mn); l=l*a+p; m=mn;
      #pragma unroll
      for(int d=0;d<32;d++) o[d]=o[d]*a+p*vs[kk][d];
    }
  }
  float sc=(1.f-g[q])/l;
  #pragma unroll
  for(int d=0;d<32;d++) O[(size_t)q*D+h*32+d]=f2bf(o[d]*sc);
}

typedef __attribute__((address_space(3))) unsigned* lds_ptr;
struct Ptrs { const char* p[7]; };
__global__ __launch_bounds__(512) void probe_k(const char* x, const char* sre, const char* sim, const char* dre, const char* dim_, const char* wqkv, const char* wo){
  extern __shared__ __attribute__((aligned(16))) char lds[];
  int lane = threadIdx.x&63, w = __builtin_amdgcn_readfirstlane(threadIdx.x>>6);
  unsigned long long t0 = __builtin_amdgcn_s_memrealtime();
  int b = blockIdx.x; int slot=0;
  #define RD(ptr, kb) for(int i=0;i<(kb)/8;i++){ size_t off=(size_t)b*(kb)*1024 + (size_t)i*8192 + w*1024 + lane*16; __builtin_amdgcn_global_load_lds((const unsigned*)((ptr)+off),(lds_ptr)(lds+((slot*8+w)&127)*1024),16,0,AUX); slot++; }
  RD(x,16) RD(wqkv,24) RD(sre,32) RD(sim,32) RD(dre,8) RD(dim_,8) RD(wo,8)
  asm volatile("s_waitcnt vmcnt(0)":::"memory");
  __syncthreads();
  unsigned long long t1 = __builtin_amdgcn_s_memrealtime();
  unsigned long long d = (t1-t0)*MULT;
  while(__builtin_amdgcn_s_memrealtime()-t1 < d) __builtin_amdgcn_s_sleep(4);
}
extern "C" void kernel_launch(void* const* d_in, const int* in_sizes, int n_in, void* d_out, int out_size, void* d_ws, size_t ws_size, hipStream_t stream){
  const u16* x=(const u16*)d_in[0]; const u16* sre=(const u16*)d_in[1]; const u16* sim=(const u16*)d_in[2];
  const u16* dre=(const u16*)d_in[3]; const u16* dim_=(const u16*)d_in[4]; const u16* wqkv=(const u16*)d_in[6]; const u16* wo=(const u16*)d_in[7]; const u16* gw=(const u16*)d_in[8];
  char* ws=(char*)d_ws;
  float* g=(float*)ws; u16* qkv=(u16*)(ws+(1<<20)); u16* P=(u16*)(ws+(16<<20)); u16* Q=(u16*)(ws+(24<<20)); u16* O=(u16*)(ws+(32<<20));
  hipFuncSetAttribute((const void*)probe_k, hipFuncAttributeMaxDynamicSharedMemorySize, 128*1024);
  probe_k<<<256,512,128*1024,stream>>>((const char*)x,(const char*)sre,(const char*)sim,(const char*)dre,(const char*)dim_,(const char*)wqkv,(const char*)wo);
  gate_k<<<S,64,0,stream>>>(x,gw,g);
  gemm_k<false><<<dim3(3072/64,S/64),256,0,stream>>>(x,D,wqkv,3072,qkv,3072,D,nullptr,1.f);
  gemm_k<false><<<dim3(D/64,S/64),256,0,stream>>>(sre,S,x,D,P,D,S,g,1.f);
  gemm_k<false><<<dim3(D/64,S/64),256,0,stream>>>(sim,S,x,D,Q,D,S,g,-1.f);
  attn_k<<<dim3(S/256,H),256,0,stream>>>(qkv,g,O);
  final_k<<<dim3(D/64,S/64),256,0,stream>>>(P,Q,O,dre,dim_,wo,(u16*)d_out);
}
```

```cpp
#include <hip/hip_runtime.h>
#include <stdint.h>
typedef unsigned short u16;
typedef unsigned long long u64;
using bf16x8 = __attribute__((ext_vector_type(8))) short;
using f32x16 = __attribute__((ext_vector_type(16))) float;
using f32x8 = __attribute__((ext_vector_type(8))) float;
using f32x4 = __attribute__((ext_vector_type(4))) float;
using u32x4 = __attribute__((ext_vector_type(4))) unsigned;
using u32x16 = __attribute__((ext_vector_type(16))) unsigned;
typedef __bf16 bf16x2_t __attribute__((ext_vector_type(2)));
typedef float f32x2_t __attribute__((ext_vector_type(2)));
#define GAS __attribute__((address_space(1)))
#define MFMA16(a, b, c) __builtin_amdgcn_mfma_f32_16x16x32_bf16(a, b, c, 0, 0, 0)
#define PRIO_A1 2
#define PRIO_A2 2
#define PRIO_B1 0
#define PRIO_B2 0
#define BSL0 96
#define PRIO_B3 0
#define RLX __ATOMIC_RELAXED, __HIP_MEMORY_SCOPE_AGENT
#define WT 17

constexpr unsigned WS_QF = 0, WS_KV = 4u << 20, WS_FF = 12u << 20, WS_O = 22u << 20, WS_G = 26u << 20, WS_XB = 27u << 20, WS_END = 31u << 20;
constexpr unsigned WS_FA = WS_G + 65536, WS_FB = WS_FA + 8192, WS_FO = WS_FB + 8192, WS_FW = WS_FO + 8192, WS_FX = WS_FW + 8192, WS_EP = WS_FX + 8192;
constexpr int LDS_BYTES = 81920;
constexpr float QSCALE = 0.17677669529663687f * 1.4426950408889634f;

__device__ __forceinline__ unsigned pk2(float lo, float hi) { f32x2_t t = {lo, hi}; return __builtin_bit_cast(unsigned, __builtin_convertvector(t, bf16x2_t)); }
__device__ __forceinline__ float bf_lo(unsigned w) { return __uint_as_float(w << 16); }
__device__ __forceinline__ float bf_hi(unsigned w) { return __uint_as_float(w & 0xffff0000u); }
__device__ __forceinline__ float rbf(float f) { return bf_lo(pk2(f, f)); }
__device__ __forceinline__ float dot2(unsigned a, unsigned b, float c) { return __builtin_amdgcn_fdot2_f32_bf16(__builtin_bit_cast(bf16x2_t, a), __builtin_bit_cast(bf16x2_t, b), c, false); }
__device__ __forceinline__ u32x4 pack44(const f32x16& a, int i, const f32x16& b, int j) {
    u32x4 w; w[0] = pk2(a[i], a[i + 1]); w[1] = pk2(a[i + 2], a[i + 3]); w[2] = pk2(b[j], b[j + 1]); w[3] = pk2(b[j + 2], b[j + 3]); return w; }
__device__ __forceinline__ float mul1(float a, float s) { float r; asm("v_mul_f32 %0, %1, %2" : "=v"(r) : "v"(a), "v"(s)); return r; }
__device__ __forceinline__ u32x4 pack44(const f32x16& a, int i, const f32x16& b, int j, float s) {
    u32x4 w; w[0] = pk2(mul1(a[i], s), mul1(a[i + 1], s)); w[1] = pk2(mul1(a[i + 2], s), mul1(a[i + 3], s)); w[2] = pk2(mul1(b[j], s), mul1(b[j + 1], s)); w[3] = pk2(mul1(b[j + 2], s), mul1(b[j + 3], s)); return w; }
#define SLP0 16
#define SLP1 16
__device__ __forceinline__ void wait_flags(const char* p, u64 tag) {
    for (unsigned spin = 0; spin < 0x80000u; ++spin) {
        if (__all(__hip_atomic_load((GAS const u64*)p, RLX) == tag)) break;
        __builtin_amdgcn_s_sleep(SLP1);
    }
    asm volatile("" ::: "memory");
}

__device__ __forceinline__ u32x4 attn_slow(__amdgpu_buffer_rsrc_t rs, unsigned kv, bf16x8 q) {
    f32x4 o0 = {}, o1 = {}; float m = -INFINITY, l = 0.f;
#pragma unroll 1
    for (int t = 0; t < 64; ++t) {
        auto ld = [&](int i) { return __builtin_bit_cast(bf16x8, __builtin_amdgcn_raw_buffer_load_b128(rs, kv + t * 4096 + i * 1024, 0, 16)); };
        const bf16x8 k0 = ld(0), k1 = ld(1), v0 = ld(2), v1 = ld(3);
        const f32x4 z = {}; f32x4 s0 = MFMA16(k0, q, z), s1 = MFMA16(k1, q, z);
        float mx = fmaxf(fmaxf(fmaxf(s0[0], s0[1]), fmaxf(s0[2], s0[3])), fmaxf(fmaxf(s1[0], s1[1]), fmaxf(s1[2], s1[3])));
        mx = fmaxf(mx, __shfl_xor(mx, 16)); mx = fmaxf(mx, __shfl_xor(mx, 32));
        const float mn = fmaxf(m, mx), a = __builtin_amdgcn_exp2f(m - mn); m = mn;
#pragma unroll
        for (int i = 0; i < 4; ++i) { s0[i] = __builtin_amdgcn_exp2f(s0[i] - mn); s1[i] = __builtin_amdgcn_exp2f(s1[i] - mn); o0[i] *= a; o1[i] *= a; }
        const u32x4 p = {pk2(s0[0], s0[1]), pk2(s0[2], s0[3]), pk2(s1[0], s1[1]), pk2(s1[2], s1[3])};
        float ps = 0.f;
#pragma unroll
        for (int i = 0; i < 4; ++i) ps += bf_lo(p[i]) + bf_hi(p[i]);
        l = l * a + ps;
        o0 = MFMA16(v0, __builtin_bit_cast(bf16x8, p), o0); o1 = MFMA16(v1, __builtin_bit_cast(bf16x8, p), o1);
    }
    l += __shfl_xor(l, 16); l += __shfl_xor(l, 32);
    const float f = 1.f / l;
    return u32x4{pk2(o0[0] * f, o0[1] * f), pk2(o0[2] * f, o0[3] * f), pk2(o1[0] * f, o1[1] * f), pk2(o1[2] * f, o1[3] * f)};
}

__global__ __launch_bounds__(256, 2) void k_all(const u16*, const u16*, const u16*, const u16*, const u16*, const u16*, const u16*, const u16*, char*, u16*) {
    extern __shared__ __attribute__((aligned(16))) char lds[];
    u32x16 ka; u32x4 kb;
    asm volatile("s_load_dwordx16 %0, %2, 0x0\n\ts_load_dwordx4 %1, %2, 0x40" : "=&{s[64:79]}"(ka), "=&{s[80:83]}"(kb) : "s"(__builtin_amdgcn_kernarg_segment_ptr()));
    const unsigned lds0 = (unsigned)(uintptr_t)lds;
    const bool roleA = blockIdx.x < 256;
#define UNIFORM(bid, tid) \
    const int w = __builtin_amdgcn_readfirstlane(tid >> 6), xcd = bid & 7, jj = (bid & 255) >> 3; \
    const int rb = 8 * (xcd & 1) + (jj & 7), cg = 4 * (xcd >> 1) + (jj >> 3), m = w >> 1, c = w & 1, rblk1 = 2 * rb + m; \
    const unsigned cX = lds0 + w * 4096, cM = cX, cW = lds0 + 32768 + w * 1024, cX2 = lds0 + m * 8192 + c * 2048, cW2 = cX2 + 32768;
    auto fsw = [](int row) { return 4 * ((row >> 4) & 1) + 2 * ((row >> 1) & 1); };
#define SRC_A(lane) \
    auto srcrow = [&](int i) { const int row = 32 * w + 8 * i + (lane >> 3); return (unsigned)((128 * rb + row) * 2048 + (((lane & 7) ^ fsw(row)) * 16) - 1024 * i); }; \
    const unsigned vx0 = srcrow(0), vx1 = srcrow(1), vx2 = srcrow(2), vx3 = srcrow(3); \
    const int kr = 8 * w + (lane >> 3), fkr = (((kr >> 1) & 1) << 1) | ((kr >> 3) & 1); \
    const unsigned vw0 = (unsigned)(kr * 6144 + cg * 128 + (((lane & 7) ^ (fkr << 1)) * 16));
#define SRC_B(lane) \
    const int hr = 64 * m + 16 * c + (lane >> 3); \
    unsigned vxa = (unsigned)((128 * rb + hr) * 2048 + (((lane & 7) ^ fsw(hr)) * 16)), vxb = vxa + 8 * 2048 - 1024; \
    unsigned vwa = vxa + (unsigned)(((cg & 7) - rb) * 128 * 2048), vwb = vwa + 8 * 2048 - 1024;
    {
    const int lane = threadIdx.x & 63;
    UNIFORM(blockIdx.x, threadIdx.x) SRC_A(lane) SRC_B(lane)
    asm volatile(
        "s_cmp_lt_u32 %[bid], 0x100\n\t"
        "s_waitcnt lgkmcnt(0)\n\t"
        "s_cbranch_scc0 1f\n\t"
        "s_mov_b64 s[40:41], s[64:65]\n\t"
        "s_and_b32 s41, s41, 0xffff\n\t"
        "s_mov_b32 s42, 0x400000\n\t"
        "s_mov_b32 s43, 0x20000\n\t"
        "s_mov_b64 s[44:45], s[74:75]\n\t"
        "s_and_b32 s45, s45, 0xffff\n\t"
        "s_mov_b32 s46, 0x600000\n\t"
        "s_mov_b32 s47, 0x20000\n\t"
        "s_mov_b32 s48, 0\n\t"
        "s_mov_b32 s49, 0\n\t"
        "s_mov_b32 s50, 2048\n\t"
        "s_mov_b32 s51, 4096\n\t"
        "s_add_u32 m0, %[cX], 0\n\t"
        "s_nop 0\n\t"
        "buffer_load_dwordx4 %[vx0], s[40:43], s48 offen offset:0 lds\n\t"
        "buffer_load_dwordx4 %[vx1], s[40:43], s48 offen offset:1024 lds\n\t"
        "buffer_load_dwordx4 %[vx2], s[40:43], s48 offen offset:2048 lds\n\t"
        "buffer_load_dwordx4 %[vx3], s[40:43], s48 offen offset:3072 lds\n\t"
        "s_add_u32 m0, %[cW], 0\n\t"
        "s_nop 0\n\t"
        "buffer_load_dwordx4 %[vw0], s[44:47], s49 offen offset:0 lds\n\t"
        "s_add_u32 m0, %[cW], 4096\n\t"
        "s_nop 0\n\t"
        "buffer_load_dwordx4 %[vw0], s[44:47], s50 offen offset:0 lds\n\t"
        "s_add_u32 m0, %[cW], 8192\n\t"
        "s_nop 0\n\t"
        "buffer_load_dwordx4 %[vw0], s[44:47], s51 offen offset:0 lds\n\t"
        "s_add_u32 s48, s48, 128\n\t"
        "s_add_u32 s49, s49, 196608\n\t"
        "s_add_u32 s50, s50, 196608\n\t"
        "s_add_u32 s51, s51, 196608\n\t"
        "s_add_u32 m0, %[cW], 12288\n\t"
        "s_nop 0\n\t"
        "buffer_load_dwordx4 %[vw0], s[44:47], s49 offen offset:0 lds\n\t"
        "s_add_u32 m0, %[cW], 16384\n\t"
        "s_nop 0\n\t"
        "buffer_load_dwordx4 %[vw0], s[44:47], s50 offen offset:0 lds\n\t"
        "s_add_u32 m0, %[cW], 20480\n\t"
        "s_nop 0\n\t"
        "buffer_load_dwordx4 %[vw0], s[44:47], s51 offen offset:0 lds\n\t"
        "s_add_u32 s49, s49, 196608\n\t"
        "s_add_u32 s50, s50, 196608\n\t"
        "s_add_u32 s51, s51, 196608\n\t"
        "s_add_u32 m0, %[cX], 16384\n\t"
        "s_nop 0\n\t"
        "buffer_load_dwordx4 %[vx0], s[40:43], s48 offen offset:0 lds\n\t"
        "buffer_load_dwordx4 %[vx1], s[40:43], s48 offen offset:1024 lds\n\t"
        "buffer_load_dwordx4 %[vx2], s[40:43], s48 offen offset:2048 lds\n\t"
        "buffer_load_dwordx4 %[vx3], s[40:43], s48 offen offset:3072 lds\n\t"
        "s_add_u32 m0, %[cW], 24576\n\t"
        "s_nop 0\n\t"
        "buffer_load_dwordx4 %[vw0], s[44:47], s49 offen offset:0 lds\n\t"
        "s_add_u32 m0, %[cW], 28672\n\t"
        "s_nop 0\n\t"
        "buffer_load_dwordx4 %[vw0], s[44:47], s50 offen offset:0 lds\n\t"
        "s_add_u32 m0, %[cW], 32768\n\t"
        "s_nop 0\n\t"
        "buffer_load_dwordx4 %[vw0], s[44:47], s51 offen offset:0 lds\n\t"
        "s_add_u32 s48, s48, 128\n\t"
        "s_add_u32 s49, s49, 196608\n\t"
        "s_add_u32 s50, s50, 196608\n\t"
        "s_add_u32 s51, s51, 196608\n\t"
        "s_add_u32 m0, %[cW], 36864\n\t"
        "s_nop 0\n\t"
        "buffer_load_dwordx4 %[vw0], s[44:47], s49 offen offset:0 lds\n\t"
        "s_add_u32 m0, %[cW], 40960\n\t"
        "s_nop 0\n\t"
        "buffer_load_dwordx4 %[vw0], s[44:47], s50 offen offset:0 lds\n\t"
        "s_add_u32 m0, %[cW], 45056\n\t"
        "s_nop 0\n\t"
        "buffer_load_dwordx4 %[vw0], s[44:47], s51 offen offset:0 lds\n\t"
        "s_add_u32 s49, s49, 196608\n\t"
        "s_add_u32 s50, s50, 196608\n\t"
        "s_add_u32 s51, s51, 196608\n\t"
        "s_branch 2f\n\t"
        "1:\n\t"
        "s_mov_b64 s[40:41], s[64:65]\n\t"
        "s_and_b32 s41, s41, 0xffff\n\t"
        "s_mov_b32 s42, 0x400000\n\t"
        "s_mov_b32 s43, 0x20000\n\t"
        "s_bitcmp1_b32 %[bid], 2\n\t"
        "s_cselect_b64 s[44:45], s[72:73], s[70:71]\n\t"
        "s_and_b32 s45, s45, 0xffff\n\t"
        "s_mov_b32 s46, 0x200000\n\t"
        "s_mov_b32 s47, 0x20000\n\t"
        "s_mov_b32 s48, 0x10000\n\t"
        "s_mov_b32 s49, 0x10080\n\t"
        "s_movk_i32 s50, 0x80\n\t"
        "s_add_u32 m0, %[cX2], 0\n\t"
        "s_nop 0\n\t"
        "buffer_load_dwordx4 %[vxa], s[40:43], 0 offen offset:0 lds\n\t"
        "buffer_load_dwordx4 %[vxb], s[40:43], 0 offen offset:1024 lds\n\t"
        "s_add_u32 m0, %[cW2], 0\n\t"
        "s_nop 0\n\t"
        "buffer_load_dwordx4 %[vwa], s[44:47], 0 offen offset:0 lds\n\t"
        "buffer_load_dwordx4 %[vwb], s[44:47], 0 offen offset:1024 lds\n\t"
        "s_add_u32 m0, %[cW2], 4096\n\t"
        "s_nop 0\n\t"
        "buffer_load_dwordx4 %[vwa], s[44:47], s48 offen offset:0 lds\n\t"
        "buffer_load_dwordx4 %[vwb], s[44:47], s48 offen offset:1024 lds\n\t"
        "s_add_u32 m0, %[cX2], 4096\n\t"
        "s_nop 0\n\t"
        "buffer_load_dwordx4 %[vxa], s[40:43], s48 offen offset:0 lds\n\t"
        "buffer_load_dwordx4 %[vxb], s[40:43], s48 offen offset:1024 lds\n\t"
        "s_add_u32 m0, %[cW2], 16384\n\t"
        "s_nop 0\n\t"
        "buffer_load_dwordx4 %[vwa], s[44:47], s50 offen offset:0 lds\n\t"
        "buffer_load_dwordx4 %[vwb], s[44:47], s50 offen offset:1024 lds\n\t"
        "s_add_u32 m0, %[cX2], 16384\n\t"
        "s_nop 0\n\t"
        "buffer_load_dwordx4 %[vxa], s[40:43], s50 offen offset:0 lds\n\t"
        "buffer_load_dwordx4 %[vxb], s[40:43], s50 offen offset:1024 lds\n\t"
        "s_add_u32 m0, %[cW2], 20480\n\t"
        "s_nop 0\n\t"
        "buffer_load_dwordx4 %[vwa], s[44:47], s49 offen offset:0 lds\n\t"
        "buffer_load_dwordx4 %[vwb], s[44:47], s49 offen offset:1024 lds\n\t"
        "s_add_u32 m0, %[cX2], 20480\n\t"
        "s_nop 0\n\t"
        "buffer_load_dwordx4 %[vxa], s[40:43], s49 offen offset:0 lds\n\t"
        "buffer_load_dwordx4 %[vxb], s[40:43], s49 offen offset:1024 lds\n\t"
        "2:\n\t"
        : "+{s[64:79]}"(ka), "+{s[80:83]}"(kb)
        : [vx0]"v"(vx0), [vx1]"v"(vx1), [vx2]"v"(vx2), [vx3]"v"(vx3), [vw0]"v"(vw0), [cX]"s"(cX), [cW]"s"(cW), [cX2]"s"(cX2), [cW2]"s"(cW2), [vxa]"v"(vxa), [vxb]"v"(vxb), [vwa]"v"(vwa), [vwb]"v"(vwb), [bid]"s"(blockIdx.x)
        : "s40", "s41", "s42", "s43", "s44", "s45", "s46", "s47", "s48", "s49", "s50", "s51", "s52", "scc", "memory");

    }
    auto arg = [&](int i) { return (const u16*)(GAS const u16*)((u64)ka[2 * i] | ((u64)ka[2 * i + 1] << 32)); };
    const u16 *x = arg(0), *wsre = arg(1), *wsim = arg(2), *wdre = arg(3), *wdim = arg(4), *wqkv = arg(5), *wo = arg(6), *gw = arg(7);
    char* ws = (char*)(GAS char*)((u64)kb[0] | ((u64)kb[1] << 32)); u16* out = (u16*)(GAS u16*)((u64)kb[2] | ((u64)kb[3] << 32));
    const u16* px = x;
#define LANE_VALUES \
    int tid = threadIdx.x, bid = blockIdx.x; asm volatile("" : "+v"(tid), "+s"(bid)); \
    UNIFORM(bid, tid) const int lane = tid & 63; \
    const int i16 = lane & 15, g4 = lane >> 4; \
    const unsigned gwv = bid * 4 + w; \
    const unsigned gl = gwv & 1023; \
    const char* pwarm = bid < 256 ? (const char*)x : (lane < 16 ? (const char*)x : lane < 40 ? (const char*)wqkv : lane < 48 ? (const char*)wdre : (const char*)wdim) \
                      + (lane < 16 ? (gl * 2 + (lane >> 3)) * 16 + (lane & 7) : lane < 40 ? gl * 24 + lane - 16 : gl * 16 + (lane & 7)) * 128; \
    [[maybe_unused]] const char* pwarm2 = pwarm + ((lane >= 16 && lane < 40) ? 1024 * 24 * 128 : 1024); \
    u64 tagv; const unsigned vep = WS_EP; \
    const auto rs = __builtin_amdgcn_make_buffer_rsrc(ws, 0, WS_END, 0x00020000);
    if (!roleA) {
        LANE_VALUES SRC_B(lane)
        const u16* pw = (cg >> 3) ? wdim : wdre;
        const int p2 = xcd >> 2, eg = cg & 7, t2 = rb, eb = 4 * eg + 2 * c + m, tb = 2 * t2 + p2, e64 = 2 * eg + c;
        const u16* pws = p2 ? wsim : wsre;
        auto fsy = [](int row) { return 4 * ((row >> 3) & 1) + 2 * ((row >> 1) & 1); };
        auto trow = [&](int row) { return (unsigned)((t2 * 128 + (row ^ (64 * p2))) * 4096 + (((lane & 7) ^ fsy(row)) * 16)); };
        const unsigned rot = rb * 256;
        f32x16 ay0, ay1, an0, an1; const unsigned vz = 0;
        const unsigned vt0 = trow(32 * w + (lane >> 3)), vt1 = trow(32 * w + 8 + (lane >> 3)) - 1024, vt2 = vt0 + 16 * 4096 - 2048, vt3 = vt1 + 16 * 4096 - 2048;
        const unsigned at0 = lds0 + i16 * 128 + ((g4 ^ fsy(i16)) * 16), at1 = lds0 + i16 * 128 + (((4 + g4) ^ fsy(i16)) * 16);
        {
            const int row = 8 * (i16 >> 2) + (i16 & 3);
            const unsigned ax0 = lds0 + (64 * m + row) * 128 + ((g4 ^ fsw(row)) * 16), ax1 = lds0 + (64 * m + row) * 128 + (((4 + g4) ^ fsw(row)) * 16);
            const unsigned aw0 = lds0 + 32768 + (64 * c + row) * 128 + ((g4 ^ fsw(row)) * 16), aw1 = lds0 + 32768 + (64 * c + row) * 128 + (((4 + g4) ^ fsw(row)) * 16);
            const unsigned vta = trow(hr), vtb = trow(hr + 8) - 1024;
            f32x16 acc0, acc1, acc2, acc3;
            __builtin_amdgcn_s_setprio(PRIO_B1);
    asm volatile(
        "global_load_dword v136, %[pfa], off\n\t"
        "s_sleep %[bsl]\n\t"
        "s_bitcmp1_b32 %[bid], 0\n\t"
        "s_cbranch_scc1 7f\n\t"
        "s_sleep 48\n\t"
        "7:\n\t"
        "global_load_dword v136, %[pfb], off\n\t"
        "global_load_dwordx2 %[tagv], %[vep], %[ws] sc1\n\t"
        "s_mov_b64 s[40:41], %[px]\n\t"
        "s_and_b32 s41, s41, 0xffff\n\t"
        "s_mov_b32 s42, 0x400000\n\t"
        "s_mov_b32 s43, 0x20000\n\t"
        "s_mov_b64 s[44:45], %[pw]\n\t"
        "s_and_b32 s45, s45, 0xffff\n\t"
        "s_mov_b32 s46, 0x200000\n\t"
        "s_mov_b32 s47, 0x20000\n\t"
        "s_mov_b32 s48, 256\n\t"
        "s_mov_b32 s49, 65792\n\t"
        "s_mov_b32 s50, 65792\n\t"
        "s_mov_b32 s51, 384\n\t"
        "s_mov_b32 s52, 384\n\t"
        "s_mov_b32 s53, 65920\n\t"
        "s_mov_b32 s54, 65920\n\t"
        "s_mov_b32 s55, 512\n\t"
        "s_mov_b32 s56, 0\n\t"
        "v_mov_b32 v192, 0\n\t"
        "v_mov_b32 v193, 0\n\t"
        "v_mov_b32 v194, 0\n\t"
        "v_mov_b32 v195, 0\n\t"
        "v_mov_b32 v196, 0\n\t"
        "v_mov_b32 v197, 0\n\t"
        "v_mov_b32 v198, 0\n\t"
        "v_mov_b32 v199, 0\n\t"
        "v_mov_b32 v200, 0\n\t"
        "v_mov_b32 v201, 0\n\t"
        "v_mov_b32 v202, 0\n\t"
        "v_mov_b32 v203, 0\n\t"
        "v_mov_b32 v204, 0\n\t"
        "v_mov_b32 v205, 0\n\t"
        "v_mov_b32 v206, 0\n\t"
        "v_mov_b32 v207, 0\n\t"
        "v_mov_b32 v208, 0\n\t"
        "v_mov_b32 v209, 0\n\t"
        "v_mov_b32 v210, 0\n\t"
        "v_mov_b32 v211, 0\n\t"
        "v_mov_b32 v212, 0\n\t"
        "v_mov_b32 v213, 0\n\t"
        "v_mov_b32 v214, 0\n\t"
        "v_mov_b32 v215, 0\n\t"
        "v_mov_b32 v216, 0\n\t"
        "v_mov_b32 v217, 0\n\t"
        "v_mov_b32 v218, 0\n\t"
        "v_mov_b32 v219, 0\n\t"
        "v_mov_b32 v220, 0\n\t"
        "v_mov_b32 v221, 0\n\t"
        "v_mov_b32 v222, 0\n\t"
        "v_mov_b32 v223, 0\n\t"
        "v_mov_b32 v224, 0\n\t"
        "v_mov_b32 v225, 0\n\t"
        "v_mov_b32 v226, 0\n\t"
        "v_mov_b32 v227, 0\n\t"
        "v_mov_b32 v228, 0\n\t"
        "v_mov_b32 v229, 0\n\t"
        "v_mov_b32 v230, 0\n\t"
        "v_mov_b32 v231, 0\n\t"
        "v_mov_b32 v232, 0\n\t"
        "v_mov_b32 v233, 0\n\t"
        "v_mov_b32 v234, 0\n\t"
        "v_mov_b32 v235, 0\n\t"
        "v_mov_b32 v236, 0\n\t"
        "v_mov_b32 v237, 0\n\t"
        "v_mov_b32 v238, 0\n\t"
        "v_mov_b32 v239, 0\n\t"
        "v_mov_b32 v240, 0\n\t"
        "v_mov_b32 v241, 0\n\t"
        "v_mov_b32 v242, 0\n\t"
        "v_mov_b32 v243, 0\n\t"
        "v_mov_b32 v244, 0\n\t"
        "v_mov_b32 v245, 0\n\t"
        "v_mov_b32 v246, 0\n\t"
        "v_mov_b32 v247, 0\n\t"
        "v_mov_b32 v248, 0\n\t"
        "v_mov_b32 v249, 0\n\t"
        "v_mov_b32 v250, 0\n\t"
        "v_mov_b32 v251, 0\n\t"
        "v_mov_b32 v252, 0\n\t"
        "v_mov_b32 v253, 0\n\t"
        "v_mov_b32 v254, 0\n\t"
        "v_mov_b32 v255, 0\n\t"
        "s_waitcnt vmcnt(13)\n\t"
        "s_barrier\n\t"
        "ds_read_b128 v[40:43], %[ax0] offset:0\n\t"
        "ds_read_b128 v[48:51], %[ax0] offset:512\n\t"
        "ds_read_b128 v[44:47], %[ax1] offset:0\n\t"
        "ds_read_b128 v[52:55], %[ax1] offset:512\n\t"
        "ds_read_b128 v[72:75], %[aw0] offset:0\n\t"
        "ds_read_b128 v[80:83], %[aw0] offset:512\n\t"
        "ds_read_b128 v[76:79], %[aw1] offset:0\n\t"
        "ds_read_b128 v[84:87], %[aw1] offset:512\n\t"
        "ds_read_b128 v[88:91], %[aw0] offset:4096\n\t"
        "ds_read_b128 v[96:99], %[aw0] offset:4608\n\t"
        "ds_read_b128 v[92:95], %[aw1] offset:4096\n\t"
        "ds_read_b128 v[100:103], %[aw1] offset:4608\n\t"
        "s_waitcnt lgkmcnt(0)\n\t"
        "s_barrier\n\t"
        "s_add_u32 m0, %[cW2], 0\n\t"
        "s_nop 0\n\t"
        "buffer_load_dwordx4 %[vwa], s[44:47], s48 offen offset:0 lds\n\t"
        "buffer_load_dwordx4 %[vwb], s[44:47], s48 offen offset:1024 lds\n\t"
        "1:\n\t"
        "s_cmp_lt_u32 s56, 7\n\t"
        "s_cbranch_scc1 2f\n\t"
        "s_mov_b64 s[40:41], %[pws]\n\t"
        "s_and_b32 s41, s41, 0xffff\n\t"
        "s_mov_b32 s42, 0x800000\n\t"
        "s_mov_b64 s[44:45], s[40:41]\n\t"
        "s_mov_b32 s46, 0x800000\n\t"
        "v_mov_b32 %[vxa], %[vta]\n\t"
        "v_mov_b32 %[vxb], %[vtb]\n\t"
        "v_mov_b32 %[vwa], %[vta]\n\t"
        "v_mov_b32 %[vwb], %[vtb]\n\t"
        "s_add_u32 s48, %[rot], 0\n\t"
        "s_and_b32 s48, s48, 4095\n\t"
        "s_add_u32 s49, %[rot], 256\n\t"
        "s_and_b32 s49, s49, 4095\n\t"
        "s_add_u32 s49, s49, 131072\n\t"
        "s_add_u32 s50, %[rot], 0\n\t"
        "s_and_b32 s50, s50, 4095\n\t"
        "s_add_u32 s50, s50, 131072\n\t"
        "s_add_u32 s51, %[rot], 384\n\t"
        "s_and_b32 s51, s51, 4095\n\t"
        "s_add_u32 s52, %[rot], 128\n\t"
        "s_and_b32 s52, s52, 4095\n\t"
        "s_add_u32 s53, %[rot], 384\n\t"
        "s_and_b32 s53, s53, 4095\n\t"
        "s_add_u32 s53, s53, 131072\n\t"
        "s_add_u32 s54, %[rot], 128\n\t"
        "s_and_b32 s54, s54, 4095\n\t"
        "s_add_u32 s54, s54, 131072\n\t"
        "s_add_u32 s55, %[rot], 256\n\t"
        "s_and_b32 s55, s55, 4095\n\t"
        "2:\n\t"
        "s_waitcnt vmcnt(8) lgkmcnt(0)\n\t"
        "s_barrier\n\t"
        "v_mfma_f32_16x16x32_bf16 v[192:195], v[40:43], v[72:75], v[192:195]\n\t"
        "ds_read_b128 v[56:59], %[ax0] offset:4096\n\t"
        "s_add_u32 m0, %[cX2], 0\n\t"
        "v_mfma_f32_16x16x32_bf16 v[196:199], v[48:51], v[72:75], v[196:199]\n\t"
        "ds_read_b128 v[64:67], %[ax0] offset:4608\n\t"
        "buffer_load_dwordx4 %[vxa], s[40:43], s48 offen offset:0 lds\n\t"
        "v_mfma_f32_16x16x32_bf16 v[212:215], v[48:51], v[80:83], v[212:215]\n\t"
        "v_mfma_f32_16x16x32_bf16 v[208:211], v[40:43], v[80:83], v[208:211]\n\t"
        "ds_read_b128 v[60:63], %[ax1] offset:4096\n\t"
        "v_mfma_f32_16x16x32_bf16 v[224:227], v[40:43], v[88:91], v[224:227]\n\t"
        "ds_read_b128 v[68:71], %[ax1] offset:4608\n\t"
        "v_mfma_f32_16x16x32_bf16 v[228:231], v[48:51], v[88:91], v[228:231]\n\t"
        "buffer_load_dwordx4 %[vxb], s[40:43], s48 offen offset:1024 lds\n\t"
        "v_mfma_f32_16x16x32_bf16 v[244:247], v[48:51], v[96:99], v[244:247]\n\t"
        "ds_read_b128 v[104:107], %[aw0] offset:16384\n\t"
        "s_add_u32 s48, s48, 256\n\t"
        "v_mfma_f32_16x16x32_bf16 v[240:243], v[40:43], v[96:99], v[240:243]\n\t"
        "v_mfma_f32_16x16x32_bf16 v[192:195], v[44:47], v[76:79], v[192:195]\n\t"
        "ds_read_b128 v[112:115], %[aw0] offset:16896\n\t"
        "s_add_u32 m0, %[cW2], 4096\n\t"
        "v_mfma_f32_16x16x32_bf16 v[196:199], v[52:55], v[76:79], v[196:199]\n\t"
        "ds_read_b128 v[108:111], %[aw1] offset:16384\n\t"
        "buffer_load_dwordx4 %[vwa], s[44:47], s49 offen offset:0 lds\n\t"
        "v_mfma_f32_16x16x32_bf16 v[212:215], v[52:55], v[84:87], v[212:215]\n\t"
        "v_mfma_f32_16x16x32_bf16 v[208:211], v[44:47], v[84:87], v[208:211]\n\t"
        "ds_read_b128 v[116:119], %[aw1] offset:16896\n\t"
        "v_mfma_f32_16x16x32_bf16 v[224:227], v[44:47], v[92:95], v[224:227]\n\t"
        "v_mfma_f32_16x16x32_bf16 v[228:231], v[52:55], v[92:95], v[228:231]\n\t"
        "buffer_load_dwordx4 %[vwb], s[44:47], s49 offen offset:1024 lds\n\t"
        "v_mfma_f32_16x16x32_bf16 v[244:247], v[52:55], v[100:103], v[244:247]\n\t"
        "s_add_u32 s49, s49, 256\n\t"
        "v_mfma_f32_16x16x32_bf16 v[240:243], v[44:47], v[100:103], v[240:243]\n\t"
        "s_waitcnt vmcnt(8) lgkmcnt(0)\n\t"
        "s_barrier\n\t"
        "v_mfma_f32_16x16x32_bf16 v[200:203], v[56:59], v[72:75], v[200:203]\n\t"
        "ds_read_b128 v[40:43], %[ax0] offset:16384\n\t"
        "s_add_u32 m0, %[cX2], 4096\n\t"
        "v_mfma_f32_16x16x32_bf16 v[204:207], v[64:67], v[72:75], v[204:207]\n\t"
        "ds_read_b128 v[48:51], %[ax0] offset:16896\n\t"
        "buffer_load_dwordx4 %[vxa], s[40:43], s50 offen offset:0 lds\n\t"
        "v_mfma_f32_16x16x32_bf16 v[220:223], v[64:67], v[80:83], v[220:223]\n\t"
        "v_mfma_f32_16x16x32_bf16 v[216:219], v[56:59], v[80:83], v[216:219]\n\t"
        "ds_read_b128 v[44:47], %[ax1] offset:16384\n\t"
        "v_mfma_f32_16x16x32_bf16 v[232:235], v[56:59], v[88:91], v[232:235]\n\t"
        "ds_read_b128 v[52:55], %[ax1] offset:16896\n\t"
        "v_mfma_f32_16x16x32_bf16 v[236:239], v[64:67], v[88:91], v[236:239]\n\t"
        "buffer_load_dwordx4 %[vxb], s[40:43], s50 offen offset:1024 lds\n\t"
        "v_mfma_f32_16x16x32_bf16 v[252:255], v[64:67], v[96:99], v[252:255]\n\t"
        "ds_read_b128 v[120:123], %[aw0] offset:20480\n\t"
        "s_add_u32 s50, s50, 256\n\t"
        "v_mfma_f32_16x16x32_bf16 v[248:251], v[56:59], v[96:99], v[248:251]\n\t"
        "v_mfma_f32_16x16x32_bf16 v[200:203], v[60:63], v[76:79], v[200:203]\n\t"
        "ds_read_b128 v[128:131], %[aw0] offset:20992\n\t"
        "s_add_u32 m0, %[cW2], 16384\n\t"
        "v_mfma_f32_16x16x32_bf16 v[204:207], v[68:71], v[76:79], v[204:207]\n\t"
        "ds_read_b128 v[124:127], %[aw1] offset:20480\n\t"
        "buffer_load_dwordx4 %[vwa], s[44:47], s51 offen offset:0 lds\n\t"
        "v_mfma_f32_16x16x32_bf16 v[220:223], v[68:71], v[84:87], v[220:223]\n\t"
        "v_mfma_f32_16x16x32_bf16 v[216:219], v[60:63], v[84:87], v[216:219]\n\t"
        "ds_read_b128 v[132:135], %[aw1] offset:20992\n\t"
        "v_mfma_f32_16x16x32_bf16 v[232:235], v[60:63], v[92:95], v[232:235]\n\t"
        "v_mfma_f32_16x16x32_bf16 v[236:239], v[68:71], v[92:95], v[236:239]\n\t"
        "buffer_load_dwordx4 %[vwb], s[44:47], s51 offen offset:1024 lds\n\t"
        "v_mfma_f32_16x16x32_bf16 v[252:255], v[68:71], v[100:103], v[252:255]\n\t"
        "s_add_u32 s51, s51, 256\n\t"
        "v_mfma_f32_16x16x32_bf16 v[248:251], v[60:63], v[100:103], v[248:251]\n\t"
        "s_waitcnt vmcnt(8) lgkmcnt(0)\n\t"
        "s_barrier\n\t"
        "v_mfma_f32_16x16x32_bf16 v[192:195], v[40:43], v[104:107], v[192:195]\n\t"
        "ds_read_b128 v[56:59], %[ax0] offset:20480\n\t"
        "s_add_u32 m0, %[cX2], 16384\n\t"
        "v_mfma_f32_16x16x32_bf16 v[196:199], v[48:51], v[104:107], v[196:199]\n\t"
        "ds_read_b128 v[64:67], %[ax0] offset:20992\n\t"
        "buffer_load_dwordx4 %[vxa], s[40:43], s52 offen offset:0 lds\n\t"
        "v_mfma_f32_16x16x32_bf16 v[212:215], v[48:51], v[112:115], v[212:215]\n\t"
        "v_mfma_f32_16x16x32_bf16 v[208:211], v[40:43], v[112:115], v[208:211]\n\t"
        "ds_read_b128 v[60:63], %[ax1] offset:20480\n\t"
        "v_mfma_f32_16x16x32_bf16 v[224:227], v[40:43], v[120:123], v[224:227]\n\t"
        "ds_read_b128 v[68:71], %[ax1] offset:20992\n\t"
        "v_mfma_f32_16x16x32_bf16 v[228:231], v[48:51], v[120:123], v[228:231]\n\t"
        "buffer_load_dwordx4 %[vxb], s[40:43], s52 offen offset:1024 lds\n\t"
        "v_mfma_f32_16x16x32_bf16 v[244:247], v[48:51], v[128:131], v[244:247]\n\t"
        "ds_read_b128 v[72:75], %[aw0] offset:0\n\t"
        "s_add_u32 s52, s52, 256\n\t"
        "v_mfma_f32_16x16x32_bf16 v[240:243], v[40:43], v[128:131], v[240:243]\n\t"
        "v_mfma_f32_16x16x32_bf16 v[192:195], v[44:47], v[108:111], v[192:195]\n\t"
        "ds_read_b128 v[80:83], %[aw0] offset:512\n\t"
        "s_add_u32 m0, %[cW2], 20480\n\t"
        "v_mfma_f32_16x16x32_bf16 v[196:199], v[52:55], v[108:111], v[196:199]\n\t"
        "ds_read_b128 v[76:79], %[aw1] offset:0\n\t"
        "buffer_load_dwordx4 %[vwa], s[44:47], s53 offen offset:0 lds\n\t"
        "v_mfma_f32_16x16x32_bf16 v[212:215], v[52:55], v[116:119], v[212:215]\n\t"
        "v_mfma_f32_16x16x32_bf16 v[208:211], v[44:47], v[116:119], v[208:211]\n\t"
        "ds_read_b128 v[84:87], %[aw1] offset:512\n\t"
        "v_mfma_f32_16x16x32_bf16 v[224:227], v[44:47], v[124:127], v[224:227]\n\t"
        "v_mfma_f32_16x16x32_bf16 v[228:231], v[52:55], v[124:127], v[228:231]\n\t"
        "buffer_load_dwordx4 %[vwb], s[44:47], s53 offen offset:1024 lds\n\t"
        "v_mfma_f32_16x16x32_bf16 v[244:247], v[52:55], v[132:135], v[244:247]\n\t"
        "s_add_u32 s53, s53, 256\n\t"
        "v_mfma_f32_16x16x32_bf16 v[240:243], v[44:47], v[132:135], v[240:243]\n\t"
        "s_waitcnt vmcnt(8) lgkmcnt(0)\n\t"
        "s_barrier\n\t"
        "v_mfma_f32_16x16x32_bf16 v[200:203], v[56:59], v[104:107], v[200:203]\n\t"
        "ds_read_b128 v[40:43], %[ax0] offset:0\n\t"
        "s_add_u32 m0, %[cX2], 20480\n\t"
        "v_mfma_f32_16x16x32_bf16 v[204:207], v[64:67], v[104:107], v[204:207]\n\t"
        "ds_read_b128 v[48:51], %[ax0] offset:512\n\t"
        "buffer_load_dwordx4 %[vxa], s[40:43], s54 offen offset:0 lds\n\t"
        "v_mfma_f32_16x16x32_bf16 v[220:223], v[64:67], v[112:115], v[220:223]\n\t"
        "v_mfma_f32_16x16x32_bf16 v[216:219], v[56:59], v[112:115], v[216:219]\n\t"
        "ds_read_b128 v[44:47], %[ax1] offset:0\n\t"
        "v_mfma_f32_16x16x32_bf16 v[232:235], v[56:59], v[120:123], v[232:235]\n\t"
        "ds_read_b128 v[52:55], %[ax1] offset:512\n\t"
        "v_mfma_f32_16x16x32_bf16 v[236:239], v[64:67], v[120:123], v[236:239]\n\t"
        "buffer_load_dwordx4 %[vxb], s[40:43], s54 offen offset:1024 lds\n\t"
        "v_mfma_f32_16x16x32_bf16 v[252:255], v[64:67], v[128:131], v[252:255]\n\t"
        "ds_read_b128 v[88:91], %[aw0] offset:4096\n\t"
        "s_add_u32 s54, s54, 256\n\t"
        "v_mfma_f32_16x16x32_bf16 v[248:251], v[56:59], v[128:131], v[248:251]\n\t"
        "v_mfma_f32_16x16x32_bf16 v[200:203], v[60:63], v[108:111], v[200:203]\n\t"
        "ds_read_b128 v[96:99], %[aw0] offset:4608\n\t"
        "s_add_u32 m0, %[cW2], 0\n\t"
        "v_mfma_f32_16x16x32_bf16 v[204:207], v[68:71], v[108:111], v[204:207]\n\t"
        "ds_read_b128 v[92:95], %[aw1] offset:4096\n\t"
        "buffer_load_dwordx4 %[vwa], s[44:47], s55 offen offset:0 lds\n\t"
        "v_mfma_f32_16x16x32_bf16 v[220:223], v[68:71], v[116:119], v[220:223]\n\t"
        "v_mfma_f32_16x16x32_bf16 v[216:219], v[60:63], v[116:119], v[216:219]\n\t"
        "ds_read_b128 v[100:103], %[aw1] offset:4608\n\t"
        "v_mfma_f32_16x16x32_bf16 v[232:235], v[60:63], v[124:127], v[232:235]\n\t"
        "v_mfma_f32_16x16x32_bf16 v[236:239], v[68:71], v[124:127], v[236:239]\n\t"
        "buffer_load_dwordx4 %[vwb], s[44:47], s55 offen offset:1024 lds\n\t"
        "v_mfma_f32_16x16x32_bf16 v[252:255], v[68:71], v[132:135], v[252:255]\n\t"
        "s_add_u32 s55, s55, 256\n\t"
        "v_mfma_f32_16x16x32_bf16 v[248:251], v[60:63], v[132:135], v[248:251]\n\t"
        "s_add_u32 s56, s56, 1\n\t"
        "s_cmp_lt_u32 s56, 8\n\t"
        "s_cbranch_scc1 1b\n\t"
        "s_waitcnt lgkmcnt(0)\n\t"
        "s_nop 7\n\t"
        : "=&{v[192:207]}"(acc0), "=&{v[208:223]}"(acc1), "=&{v[224:239]}"(acc2), "=&{v[240:255]}"(acc3), [tagv]"=&v"(tagv), [vxa]"+v"(vxa), [vxb]"+v"(vxb), [vwa]"+v"(vwa), [vwb]"+v"(vwb)
        : [ax0]"v"(ax0), [ax1]"v"(ax1), [aw0]"v"(aw0), [aw1]"v"(aw1), [vta]"v"(vta), [vtb]"v"(vtb), [pfb]"v"(pwarm2), [cX2]"s"(cX2), [cW2]"s"(cW2), [px]"s"(px), [pw]"s"(pw), [pfa]"v"(pwarm), [vep]"v"(vep), [ws]"s"(ws), [pws]"s"(pws), [rot]"s"(rot), [bsl]"n"(BSL0), [bid]"s"(bid)
        : "v40", "v41", "v42", "v43", "v44", "v45", "v46", "v47", "v48", "v49", "v50", "v51", "v52", "v53", "v54", "v55", "v56", "v57", "v58", "v59", "v60", "v61", "v62", "v63", "v64", "v65", "v66", "v67", "v68", "v69", "v70", "v71", "v72", "v73", "v74", "v75", "v76", "v77", "v78", "v79", "v80", "v81", "v82", "v83", "v84", "v85", "v86", "v87", "v88", "v89", "v90", "v91", "v92", "v93", "v94", "v95", "v96", "v97", "v98", "v99", "v100", "v101", "v102", "v103", "v104", "v105", "v106", "v107", "v108", "v109", "v110", "v111", "v112", "v113", "v114", "v115", "v116", "v117", "v118", "v119", "v120", "v121", "v122", "v123", "v124", "v125", "v126", "v127", "v128", "v129", "v130", "v131", "v132", "v133", "v134", "v135", "v136", "s40", "s41", "s42", "s43", "s44", "s45", "s46", "s47", "s48", "s49", "s50", "s51", "s52", "s53", "s54", "s55", "s56", "scc", "memory");
            tagv += 1;
            __builtin_amdgcn_s_setprio(3);
            const unsigned of = WS_FF + 2 * e64 * 327680 + (p2 * 64 + 2 * rblk1) * 2048 + lane * 16;
            u32x16 fown, fsend;
#pragma unroll
            for (int gp = 0; gp < 2; ++gp) {
                const u32x4 f0 = pack44(acc0, 8 * gp, acc0, 8 * gp + 4), f1 = pack44(acc1, 8 * gp, acc1, 8 * gp + 4), f2 = pack44(acc2, 8 * gp, acc2, 8 * gp + 4), f3 = pack44(acc3, 8 * gp, acc3, 8 * gp + 4);
                __builtin_amdgcn_raw_buffer_store_b128(f0, rs, of + gp * 2048, 0, WT);
                __builtin_amdgcn_raw_buffer_store_b128(f1, rs, of + gp * 2048 + 1024, 0, WT);
                __builtin_amdgcn_raw_buffer_store_b128(f2, rs, of + 327680 + gp * 2048, 0, WT);
                __builtin_amdgcn_raw_buffer_store_b128(f3, rs, of + 327680 + gp * 2048 + 1024, 0, WT);
#pragma unroll
                for (int i = 0; i < 4; ++i) { fown[8 * gp + i] = m ? f2[i] : f0[i]; fown[8 * gp + 4 + i] = m ? f3[i] : f1[i]; fsend[8 * gp + i] = m ? f0[i] : f2[i]; fsend[8 * gp + 4 + i] = m ? f1[i] : f3[i]; } }
            __builtin_amdgcn_s_setprio(PRIO_B3);
            const unsigned atA0 = at0 + m * 16384, atA1 = at1 + m * 16384, atB0 = at0 + (1 - m) * 16384, atB1 = at1 + (1 - m) * 16384;
            const unsigned axw = lds0 + 65536 + w * 4096 + lane * 16, axr = lds0 + 65536 + (w ^ 2) * 4096 + lane * 16;
            const char* pfb = ws + WS_FB + (e64 * 64 + p2 * 32 + rblk1) * 8;
    asm volatile(
        "ds_write_b128 %[axw], v[192:195] offset:0\n\t"
        "ds_write_b128 %[axw], v[196:199] offset:1024\n\t"
        "ds_write_b128 %[axw], v[200:203] offset:2048\n\t"
        "ds_write_b128 %[axw], v[204:207] offset:3072\n\t"
        "s_mov_b64 s[40:41], %[pws]\n\t"
        "s_and_b32 s41, s41, 0xffff\n\t"
        "s_mov_b32 s42, 0x800000\n\t"
        "s_mov_b32 s43, 0x20000\n\t"
        "s_add_u32 s44, %[rot], 512\n\t"
        "s_and_b32 s44, s44, 4095\n\t"
        "s_add_u32 s45, %[rot], 640\n\t"
        "s_and_b32 s45, s45, 4095\n\t"
        "s_waitcnt vmcnt(8) lgkmcnt(0)\n\t"
        "s_barrier\n\t"
        "ds_read_b128 v[96:99], %[atA0] offset:0\n\t"
        "ds_read_b128 v[100:103], %[atA0] offset:2048\n\t"
        "ds_read_b128 v[104:107], %[atA0] offset:4096\n\t"
        "ds_read_b128 v[108:111], %[atA0] offset:6144\n\t"
        "ds_read_b128 v[112:115], %[atA0] offset:8192\n\t"
        "ds_read_b128 v[116:119], %[atA0] offset:10240\n\t"
        "ds_read_b128 v[120:123], %[atA0] offset:12288\n\t"
        "ds_read_b128 v[124:127], %[atA0] offset:14336\n\t"
        "ds_read_b128 v[176:179], %[axr] offset:0\n\t"
        "ds_read_b128 v[180:183], %[axr] offset:1024\n\t"
        "ds_read_b128 v[184:187], %[axr] offset:2048\n\t"
        "ds_read_b128 v[188:191], %[axr] offset:3072\n\t"
        "s_waitcnt lgkmcnt(4)\n\t"
        "v_mfma_f32_16x16x32_bf16 v[32:35], v[160:163], v[96:99], 0\n\t"
        "ds_read_b128 v[128:131], %[atA1] offset:0\n\t"
        "v_mfma_f32_16x16x32_bf16 v[48:51], v[164:167], v[96:99], 0\n\t"
        "ds_read_b128 v[132:135], %[atA1] offset:2048\n\t"
        "v_mfma_f32_16x16x32_bf16 v[36:39], v[160:163], v[100:103], 0\n\t"
        "v_mfma_f32_16x16x32_bf16 v[52:55], v[164:167], v[100:103], 0\n\t"
        "ds_read_b128 v[136:139], %[atA1] offset:4096\n\t"
        "v_mfma_f32_16x16x32_bf16 v[40:43], v[160:163], v[104:107], 0\n\t"
        "ds_read_b128 v[140:143], %[atA1] offset:6144\n\t"
        "v_mfma_f32_16x16x32_bf16 v[56:59], v[164:167], v[104:107], 0\n\t"
        "v_mfma_f32_16x16x32_bf16 v[44:47], v[160:163], v[108:111], 0\n\t"
        "ds_read_b128 v[144:147], %[atA1] offset:8192\n\t"
        "v_mfma_f32_16x16x32_bf16 v[60:63], v[164:167], v[108:111], 0\n\t"
        "v_mfma_f32_16x16x32_bf16 v[64:67], v[160:163], v[112:115], 0\n\t"
        "ds_read_b128 v[148:151], %[atA1] offset:10240\n\t"
        "v_mfma_f32_16x16x32_bf16 v[80:83], v[164:167], v[112:115], 0\n\t"
        "ds_read_b128 v[152:155], %[atA1] offset:12288\n\t"
        "v_mfma_f32_16x16x32_bf16 v[68:71], v[160:163], v[116:119], 0\n\t"
        "v_mfma_f32_16x16x32_bf16 v[84:87], v[164:167], v[116:119], 0\n\t"
        "ds_read_b128 v[156:159], %[atA1] offset:14336\n\t"
        "v_mfma_f32_16x16x32_bf16 v[72:75], v[160:163], v[120:123], 0\n\t"
        "v_mfma_f32_16x16x32_bf16 v[88:91], v[164:167], v[120:123], 0\n\t"
        "v_mfma_f32_16x16x32_bf16 v[76:79], v[160:163], v[124:127], 0\n\t"
        "v_mfma_f32_16x16x32_bf16 v[92:95], v[164:167], v[124:127], 0\n\t"
        "s_waitcnt lgkmcnt(0)\n\t"
        "v_mfma_f32_16x16x32_bf16 v[32:35], v[168:171], v[128:131], v[32:35]\n\t"
        "ds_read_b128 v[96:99], %[atB0] offset:0\n\t"
        "v_mfma_f32_16x16x32_bf16 v[48:51], v[172:175], v[128:131], v[48:51]\n\t"
        "ds_read_b128 v[100:103], %[atB0] offset:2048\n\t"
        "v_mfma_f32_16x16x32_bf16 v[36:39], v[168:171], v[132:135], v[36:39]\n\t"
        "v_mfma_f32_16x16x32_bf16 v[52:55], v[172:175], v[132:135], v[52:55]\n\t"
        "ds_read_b128 v[104:107], %[atB0] offset:4096\n\t"
        "v_mfma_f32_16x16x32_bf16 v[40:43], v[168:171], v[136:139], v[40:43]\n\t"
        "ds_read_b128 v[108:111], %[atB0] offset:6144\n\t"
        "v_mfma_f32_16x16x32_bf16 v[56:59], v[172:175], v[136:139], v[56:59]\n\t"
        "v_mfma_f32_16x16x32_bf16 v[44:47], v[168:171], v[140:143], v[44:47]\n\t"
        "ds_read_b128 v[112:115], %[atB0] offset:8192\n\t"
        "v_mfma_f32_16x16x32_bf16 v[60:63], v[172:175], v[140:143], v[60:63]\n\t"
        "v_mfma_f32_16x16x32_bf16 v[64:67], v[168:171], v[144:147], v[64:67]\n\t"
        "ds_read_b128 v[116:119], %[atB0] offset:10240\n\t"
        "v_mfma_f32_16x16x32_bf16 v[80:83], v[172:175], v[144:147], v[80:83]\n\t"
        "ds_read_b128 v[120:123], %[atB0] offset:12288\n\t"
        "v_mfma_f32_16x16x32_bf16 v[68:71], v[168:171], v[148:151], v[68:71]\n\t"
        "v_mfma_f32_16x16x32_bf16 v[84:87], v[172:175], v[148:151], v[84:87]\n\t"
        "ds_read_b128 v[124:127], %[atB0] offset:14336\n\t"
        "v_mfma_f32_16x16x32_bf16 v[72:75], v[168:171], v[152:155], v[72:75]\n\t"
        "v_mfma_f32_16x16x32_bf16 v[88:91], v[172:175], v[152:155], v[88:91]\n\t"
        "v_mfma_f32_16x16x32_bf16 v[76:79], v[168:171], v[156:159], v[76:79]\n\t"
        "v_mfma_f32_16x16x32_bf16 v[92:95], v[172:175], v[156:159], v[92:95]\n\t"
        "s_waitcnt vmcnt(0)\n\t"
        "s_mov_b64 exec, 1\n\t"
        "global_store_dwordx2 %[vz], %[tagv], %[pfb] sc1\n\t"
        "s_mov_b64 exec, -1\n\t"
        "s_waitcnt lgkmcnt(0)\n\t"
        "v_mfma_f32_16x16x32_bf16 v[32:35], v[176:179], v[96:99], v[32:35]\n\t"
        "ds_read_b128 v[128:131], %[atB1] offset:0\n\t"
        "v_mfma_f32_16x16x32_bf16 v[48:51], v[180:183], v[96:99], v[48:51]\n\t"
        "ds_read_b128 v[132:135], %[atB1] offset:2048\n\t"
        "v_mfma_f32_16x16x32_bf16 v[36:39], v[176:179], v[100:103], v[36:39]\n\t"
        "v_mfma_f32_16x16x32_bf16 v[52:55], v[180:183], v[100:103], v[52:55]\n\t"
        "ds_read_b128 v[136:139], %[atB1] offset:4096\n\t"
        "v_mfma_f32_16x16x32_bf16 v[40:43], v[176:179], v[104:107], v[40:43]\n\t"
        "ds_read_b128 v[140:143], %[atB1] offset:6144\n\t"
        "v_mfma_f32_16x16x32_bf16 v[56:59], v[180:183], v[104:107], v[56:59]\n\t"
        "v_mfma_f32_16x16x32_bf16 v[44:47], v[176:179], v[108:111], v[44:47]\n\t"
        "ds_read_b128 v[144:147], %[atB1] offset:8192\n\t"
        "v_mfma_f32_16x16x32_bf16 v[60:63], v[180:183], v[108:111], v[60:63]\n\t"
        "v_mfma_f32_16x16x32_bf16 v[64:67], v[176:179], v[112:115], v[64:67]\n\t"
        "ds_read_b128 v[148:151], %[atB1] offset:10240\n\t"
        "v_mfma_f32_16x16x32_bf16 v[80:83], v[180:183], v[112:115], v[80:83]\n\t"
        "ds_read_b128 v[152:155], %[atB1] offset:12288\n\t"
        "v_mfma_f32_16x16x32_bf16 v[68:71], v[176:179], v[116:119], v[68:71]\n\t"
        "v_mfma_f32_16x16x32_bf16 v[84:87], v[180:183], v[116:119], v[84:87]\n\t"
        "ds_read_b128 v[156:159], %[atB1] offset:14336\n\t"
        "v_mfma_f32_16x16x32_bf16 v[72:75], v[176:179], v[120:123], v[72:75]\n\t"
        "v_mfma_f32_16x16x32_bf16 v[88:91], v[180:183], v[120:123], v[88:91]\n\t"
        "v_mfma_f32_16x16x32_bf16 v[76:79], v[176:179], v[124:127], v[76:79]\n\t"
        "v_mfma_f32_16x16x32_bf16 v[92:95], v[180:183], v[124:127], v[92:95]\n\t"
        "s_waitcnt lgkmcnt(0)\n\t"
        "s_barrier\n\t"
        "s_mov_b32 m0, %[cM]\n\t"
        "v_mfma_f32_16x16x32_bf16 v[32:35], v[184:187], v[128:131], v[32:35]\n\t"
        "buffer_load_dwordx4 %[vt0], s[40:43], s44 offen offset:0 lds\n\t"
        "v_mfma_f32_16x16x32_bf16 v[48:51], v[188:191], v[128:131], v[48:51]\n\t"
        "v_mfma_f32_16x16x32_bf16 v[36:39], v[184:187], v[132:135], v[36:39]\n\t"
        "buffer_load_dwordx4 %[vt1], s[40:43], s44 offen offset:1024 lds\n\t"
        "v_mfma_f32_16x16x32_bf16 v[52:55], v[188:191], v[132:135], v[52:55]\n\t"
        "v_mfma_f32_16x16x32_bf16 v[40:43], v[184:187], v[136:139], v[40:43]\n\t"
        "buffer_load_dwordx4 %[vt2], s[40:43], s44 offen offset:2048 lds\n\t"
        "v_mfma_f32_16x16x32_bf16 v[56:59], v[188:191], v[136:139], v[56:59]\n\t"
        "v_mfma_f32_16x16x32_bf16 v[44:47], v[184:187], v[140:143], v[44:47]\n\t"
        "buffer_load_dwordx4 %[vt3], s[40:43], s44 offen offset:3072 lds\n\t"
        "v_mfma_f32_16x16x32_bf16 v[60:63], v[188:191], v[140:143], v[60:63]\n\t"
        "v_mfma_f32_16x16x32_bf16 v[64:67], v[184:187], v[144:147], v[64:67]\n\t"
        "s_add_u32 m0, %[cM], 16384\n\t"
        "v_mfma_f32_16x16x32_bf16 v[80:83], v[188:191], v[144:147], v[80:83]\n\t"
        "v_mfma_f32_16x16x32_bf16 v[68:71], v[184:187], v[148:151], v[68:71]\n\t"
        "buffer_load_dwordx4 %[vt0], s[40:43], s45 offen offset:0 lds\n\t"
        "v_mfma_f32_16x16x32_bf16 v[84:87], v[188:191], v[148:151], v[84:87]\n\t"
        "v_mfma_f32_16x16x32_bf16 v[72:75], v[184:187], v[152:155], v[72:75]\n\t"
        "buffer_load_dwordx4 %[vt1], s[40:43], s45 offen offset:1024 lds\n\t"
        "v_mfma_f32_16x16x32_bf16 v[88:91], v[188:191], v[152:155], v[88:91]\n\t"
        "v_mfma_f32_16x16x32_bf16 v[76:79], v[184:187], v[156:159], v[76:79]\n\t"
        "v_mfma_f32_16x16x32_bf16 v[92:95], v[188:191], v[156:159], v[92:95]\n\t"
        "s_nop 7\n\t"
        : "=&{v[32:47]}"(ay0), "=&{v[48:63]}"(ay1), "=&{v[64:79]}"(an0), "=&{v[80:95]}"(an1)
        : "{v[160:175]}"(fown), "{v[192:207]}"(fsend), [vt0]"v"(vt0), [vt1]"v"(vt1), [vt2]"v"(vt2), [vt3]"v"(vt3), [atA0]"v"(atA0), [atA1]"v"(atA1), [atB0]"v"(atB0), [atB1]"v"(atB1), [axw]"v"(axw), [axr]"v"(axr), [vz]"v"(vz), [tagv]"v"(tagv), [pfb]"s"(pfb), [cM]"s"(cM), [pws]"s"(pws), [rot]"s"(rot)
        : "v96", "v97", "v98", "v99", "v100", "v101", "v102", "v103", "v104", "v105", "v106", "v107", "v108", "v109", "v110", "v111", "v112", "v113", "v114", "v115", "v116", "v117", "v118", "v119", "v120", "v121", "v122", "v123", "v124", "v125", "v126", "v127", "v128", "v129", "v130", "v131", "v132", "v133", "v134", "v135", "v136", "v137", "v138", "v139", "v140", "v141", "v142", "v143", "v144", "v145", "v146", "v147", "v148", "v149", "v150", "v151", "v152", "v153", "v154", "v155", "v156", "v157", "v158", "v159", "v176", "v177", "v178", "v179", "v180", "v181", "v182", "v183", "v184", "v185", "v186", "v187", "v188", "v189", "v190", "v191", "s40", "s41", "s42", "s43", "s44", "s45", "scc", "memory");

        }
        const char* pf = ws + WS_FF + eb * 327680 + p2 * 131072;
        const unsigned vf = lane * 16;
        __builtin_amdgcn_s_setprio(0);
        __builtin_amdgcn_s_sleep(SLP0);
        if (w == 0) wait_flags(ws + WS_FB + ((2 * eg + (lane >> 5)) * 64 + p2 * 32 + (lane & 31)) * 8, tagv);
        asm volatile("s_waitcnt vmcnt(0)\n\ts_barrier" ::: "memory");
        __builtin_amdgcn_s_setprio(PRIO_B2);
        u64 fw; unsigned pre; u32x16 wf0, wf1, wf2, wf3, wf4, wf5;
        const u64 tag = (u64)(unsigned)__builtin_amdgcn_readfirstlane((unsigned)tagv) | ((u64)(unsigned)__builtin_amdgcn_readfirstlane((unsigned)(tagv >> 32)) << 32);
        const char* pfo = ws + WS_FF + eb * 327680 + 262144;
        const char* pfw = ws + WS_FW + eb * 256; const unsigned vfw = (lane & 31) * 8;
    asm volatile(
        "s_mov_b64 s[40:41], %[pws]\n\t"
        "s_and_b32 s41, s41, 0xffff\n\t"
        "s_mov_b32 s42, 8388608\n\t"
        "s_mov_b32 s43, 0x20000\n\t"
        "s_mov_b64 s[44:45], %[pf]\n\t"
        "s_and_b32 s45, s45, 0xffff\n\t"
        "s_mov_b32 s46, 131072\n\t"
        "s_mov_b32 s47, 0x20000\n\t"
        "s_mov_b64 s[48:49], s[44:45]\n\t"
        "s_mov_b32 s50, 0\n\t"
        "s_mov_b32 s51, 0x20000\n\t"
        "s_mov_b64 s[56:57], %[pfw]\n\t"
        "s_and_b32 s57, s57, 0xffff\n\t"
        "s_mov_b32 s58, 256\n\t"
        "s_mov_b32 s59, 0x20000\n\t"
        "s_add_u32 s52, %[rot], 640\n\t"
        "s_and_b32 s52, s52, 4095\n\t"
        "s_lshl_b32 s53, %[rot], 5\n\t"
        "s_add_u32 s53, s53, 8192\n\t"
        "s_and_b32 s53, s53, 0x1ffff\n\t"
        "s_mov_b32 s54, 0\n\t"
        "s_mov_b64 s[60:61], %[pwo]\n\t"
        "s_and_b32 s61, s61, 0xffff\n\t"
        "s_mov_b32 %[pre], 0\n\t"
        "buffer_load_dword v224, %[vf], s[48:51], 0 offen\n\t"
        "buffer_load_dwordx4 v[192:195], %[vf], s[44:47], s53 offen offset:0 sc1\n\t"
        "buffer_load_dword v224, %[vf], s[48:51], 0 offen\n\t"
        "buffer_load_dwordx4 v[196:199], %[vf], s[44:47], s53 offen offset:1024 sc1\n\t"
        "buffer_load_dword v224, %[vf], s[48:51], 0 offen\n\t"
        "buffer_load_dwordx4 v[200:203], %[vf], s[44:47], s53 offen offset:2048 sc1\n\t"
        "buffer_load_dword v224, %[vf], s[48:51], 0 offen\n\t"
        "buffer_load_dwordx4 v[204:207], %[vf], s[44:47], s53 offen offset:3072 sc1\n\t"
        "s_add_u32 s53, s53, 4096\n\t"
        "s_and_b32 s53, s53, 0x1ffff\n\t"
        "buffer_load_dword v224, %[vf], s[48:51], 0 offen\n\t"
        "buffer_load_dwordx4 v[208:211], %[vf], s[44:47], s53 offen offset:0 sc1\n\t"
        "buffer_load_dword v224, %[vf], s[48:51], 0 offen\n\t"
        "buffer_load_dwordx4 v[212:215], %[vf], s[44:47], s53 offen offset:1024 sc1\n\t"
        "buffer_load_dword v224, %[vf], s[48:51], 0 offen\n\t"
        "buffer_load_dwordx4 v[216:219], %[vf], s[44:47], s53 offen offset:2048 sc1\n\t"
        "buffer_load_dword v224, %[vf], s[48:51], 0 offen\n\t"
        "buffer_load_dwordx4 v[220:223], %[vf], s[44:47], s53 offen offset:3072 sc1\n\t"
        "s_add_u32 s53, s53, 4096\n\t"
        "s_and_b32 s53, s53, 0x1ffff\n\t"
        "buffer_load_dword v224, %[vf], s[48:51], 0 offen\n\t"
        "buffer_load_dwordx4 v[160:163], %[vf], s[44:47], s53 offen offset:0 sc1\n\t"
        "buffer_load_dword v224, %[vf], s[48:51], 0 offen\n\t"
        "buffer_load_dwordx4 v[164:167], %[vf], s[44:47], s53 offen offset:1024 sc1\n\t"
        "buffer_load_dword v224, %[vf], s[48:51], 0 offen\n\t"
        "buffer_load_dwordx4 v[168:171], %[vf], s[44:47], s53 offen offset:2048 sc1\n\t"
        "buffer_load_dword v224, %[vf], s[48:51], 0 offen\n\t"
        "buffer_load_dwordx4 v[172:175], %[vf], s[44:47], s53 offen offset:3072 sc1\n\t"
        "s_add_u32 s53, s53, 4096\n\t"
        "s_and_b32 s53, s53, 0x1ffff\n\t"
        "buffer_load_dword v224, %[vf], s[48:51], 0 offen\n\t"
        "buffer_load_dwordx4 v[176:179], %[vf], s[44:47], s53 offen offset:0 sc1\n\t"
        "buffer_load_dword v224, %[vf], s[48:51], 0 offen\n\t"
        "buffer_load_dwordx4 v[180:183], %[vf], s[44:47], s53 offen offset:1024 sc1\n\t"
        "ds_read_b128 v[96:99], %[at0] offset:32768\n\t"
        "ds_read_b128 v[100:103], %[at0] offset:34816\n\t"
        "ds_read_b128 v[104:107], %[at0] offset:36864\n\t"
        "ds_read_b128 v[108:111], %[at0] offset:38912\n\t"
        "ds_read_b128 v[112:115], %[at0] offset:40960\n\t"
        "ds_read_b128 v[116:119], %[at0] offset:43008\n\t"
        "ds_read_b128 v[120:123], %[at0] offset:45056\n\t"
        "ds_read_b128 v[124:127], %[at0] offset:47104\n\t"
        "s_add_u32 m0, %[cM], 16384\n\t"
        "s_branch 2f\n\t"
        "1:\n\t"
        "s_waitcnt vmcnt(25) lgkmcnt(0)\n\t"
        "v_mfma_f32_16x16x32_bf16 v[32:35], v[160:163], v[96:99], v[32:35]\n\t"
        "ds_read_b128 v[128:131], %[at1] offset:0\n\t"
        "v_mfma_f32_16x16x32_bf16 v[48:51], v[164:167], v[96:99], v[48:51]\n\t"
        "ds_read_b128 v[132:135], %[at1] offset:2048\n\t"
        "buffer_load_dwordx4 %[vt2], s[40:43], s52 offen offset:2048 lds\n\t"
        "v_mfma_f32_16x16x32_bf16 v[52:55], v[164:167], v[100:103], v[52:55]\n\t"
        "v_mfma_f32_16x16x32_bf16 v[36:39], v[160:163], v[100:103], v[36:39]\n\t"
        "ds_read_b128 v[136:139], %[at1] offset:4096\n\t"
        "v_mfma_f32_16x16x32_bf16 v[40:43], v[160:163], v[104:107], v[40:43]\n\t"
        "ds_read_b128 v[140:143], %[at1] offset:6144\n\t"
        "v_mfma_f32_16x16x32_bf16 v[56:59], v[164:167], v[104:107], v[56:59]\n\t"
        "buffer_load_dwordx4 v[216:219], %[vf], s[44:47], s53 offen offset:2048 sc1\n\t"
        "v_mfma_f32_16x16x32_bf16 v[60:63], v[164:167], v[108:111], v[60:63]\n\t"
        "ds_read_b128 v[144:147], %[at1] offset:8192\n\t"
        "v_mfma_f32_16x16x32_bf16 v[44:47], v[160:163], v[108:111], v[44:47]\n\t"
        "v_mfma_f32_16x16x32_bf16 v[64:67], v[160:163], v[112:115], v[64:67]\n\t"
        "ds_read_b128 v[148:151], %[at1] offset:10240\n\t"
        "v_mfma_f32_16x16x32_bf16 v[80:83], v[164:167], v[112:115], v[80:83]\n\t"
        "ds_read_b128 v[152:155], %[at1] offset:12288\n\t"
        "buffer_load_dwordx4 %[vt3], s[40:43], s52 offen offset:3072 lds\n\t"
        "v_mfma_f32_16x16x32_bf16 v[84:87], v[164:167], v[116:119], v[84:87]\n\t"
        "v_mfma_f32_16x16x32_bf16 v[68:71], v[160:163], v[116:119], v[68:71]\n\t"
        "ds_read_b128 v[156:159], %[at1] offset:14336\n\t"
        "v_mfma_f32_16x16x32_bf16 v[72:75], v[160:163], v[120:123], v[72:75]\n\t"
        "v_mfma_f32_16x16x32_bf16 v[88:91], v[164:167], v[120:123], v[88:91]\n\t"
        "buffer_load_dwordx4 v[220:223], %[vf], s[44:47], s53 offen offset:3072 sc1\n\t"
        "s_cmp_eq_u32 s54, 7\n\t"
        "s_cselect_b32 s46, 0, s46\n\t"
        "s_cselect_b32 s42, 0, s42\n\t"
        "s_cselect_b32 %[pre], s55, 0\n\t"
        "s_cmp_lg_u32 %[pre], 0\n\t"
        "s_cselect_b64 s[44:45], s[60:61], s[44:45]\n\t"
        "s_cselect_b32 s46, 0x10000, s46\n\t"
        "s_cselect_b32 s53, 0xfffff000, s53\n\t"
        "v_mfma_f32_16x16x32_bf16 v[92:95], v[164:167], v[124:127], v[92:95]\n\t"
        "s_add_u32 s52, s52, 128\n\t"
        "s_add_u32 s53, s53, 4096\n\t"
        "v_mfma_f32_16x16x32_bf16 v[76:79], v[160:163], v[124:127], v[76:79]\n\t"
        "s_and_b32 s52, s52, 4095\n\t"
        "s_and_b32 s53, s53, 0x1ffff\n\t"
        "s_waitcnt vmcnt(17) lgkmcnt(0)\n\t"
        "s_barrier\n\t"
        "s_add_u32 m0, %[cM], 0\n\t"
        "v_mfma_f32_16x16x32_bf16 v[32:35], v[168:171], v[128:131], v[32:35]\n\t"
        "ds_read_b128 v[96:99], %[at0] offset:16384\n\t"
        "v_mfma_f32_16x16x32_bf16 v[48:51], v[172:175], v[128:131], v[48:51]\n\t"
        "ds_read_b128 v[100:103], %[at0] offset:18432\n\t"
        "buffer_load_dwordx4 %[vt0], s[40:43], s52 offen offset:0 lds\n\t"
        "v_mfma_f32_16x16x32_bf16 v[52:55], v[172:175], v[132:135], v[52:55]\n\t"
        "v_mfma_f32_16x16x32_bf16 v[36:39], v[168:171], v[132:135], v[36:39]\n\t"
        "ds_read_b128 v[104:107], %[at0] offset:20480\n\t"
        "v_mfma_f32_16x16x32_bf16 v[40:43], v[168:171], v[136:139], v[40:43]\n\t"
        "ds_read_b128 v[108:111], %[at0] offset:22528\n\t"
        "v_mfma_f32_16x16x32_bf16 v[56:59], v[172:175], v[136:139], v[56:59]\n\t"
        "buffer_load_dwordx4 v[160:163], %[vf], s[44:47], s53 offen offset:0 sc1\n\t"
        "v_mfma_f32_16x16x32_bf16 v[60:63], v[172:175], v[140:143], v[60:63]\n\t"
        "ds_read_b128 v[112:115], %[at0] offset:24576\n\t"
        "v_mfma_f32_16x16x32_bf16 v[44:47], v[168:171], v[140:143], v[44:47]\n\t"
        "v_mfma_f32_16x16x32_bf16 v[64:67], v[168:171], v[144:147], v[64:67]\n\t"
        "ds_read_b128 v[116:119], %[at0] offset:26624\n\t"
        "v_mfma_f32_16x16x32_bf16 v[80:83], v[172:175], v[144:147], v[80:83]\n\t"
        "ds_read_b128 v[120:123], %[at0] offset:28672\n\t"
        "buffer_load_dwordx4 %[vt1], s[40:43], s52 offen offset:1024 lds\n\t"
        "v_mfma_f32_16x16x32_bf16 v[84:87], v[172:175], v[148:151], v[84:87]\n\t"
        "v_mfma_f32_16x16x32_bf16 v[68:71], v[168:171], v[148:151], v[68:71]\n\t"
        "ds_read_b128 v[124:127], %[at0] offset:30720\n\t"
        "s_cmp_lg_u32 %[pre], 0\n\t"
        "s_cbranch_scc0 5f\n\t"
        "s_mov_b32 s62, 16384\n\t"
        "buffer_load_dwordx4 v[224:227], %[vf], s[44:47], s62 offen offset:0 sc1\n\t"
        "buffer_load_dwordx4 v[228:231], %[vf], s[44:47], s62 offen offset:1024 sc1\n\t"
        "buffer_load_dwordx4 v[232:235], %[vf], s[44:47], s62 offen offset:2048 sc1\n\t"
        "buffer_load_dwordx4 v[236:239], %[vf], s[44:47], s62 offen offset:3072 sc1\n\t"
        "5:\n\t"
        "v_mfma_f32_16x16x32_bf16 v[72:75], v[168:171], v[152:155], v[72:75]\n\t"
        "v_mfma_f32_16x16x32_bf16 v[88:91], v[172:175], v[152:155], v[88:91]\n\t"
        "buffer_load_dwordx4 v[164:167], %[vf], s[44:47], s53 offen offset:1024 sc1\n\t"
        "v_mfma_f32_16x16x32_bf16 v[92:95], v[172:175], v[156:159], v[92:95]\n\t"
        "v_mfma_f32_16x16x32_bf16 v[76:79], v[168:171], v[156:159], v[76:79]\n\t"
        "s_waitcnt vmcnt(25) lgkmcnt(0)\n\t"
        "v_mfma_f32_16x16x32_bf16 v[32:35], v[176:179], v[96:99], v[32:35]\n\t"
        "ds_read_b128 v[128:131], %[at1] offset:16384\n\t"
        "v_mfma_f32_16x16x32_bf16 v[48:51], v[180:183], v[96:99], v[48:51]\n\t"
        "ds_read_b128 v[132:135], %[at1] offset:18432\n\t"
        "buffer_load_dwordx4 %[vt2], s[40:43], s52 offen offset:2048 lds\n\t"
        "v_mfma_f32_16x16x32_bf16 v[52:55], v[180:183], v[100:103], v[52:55]\n\t"
        "v_mfma_f32_16x16x32_bf16 v[36:39], v[176:179], v[100:103], v[36:39]\n\t"
        "ds_read_b128 v[136:139], %[at1] offset:20480\n\t"
        "v_mfma_f32_16x16x32_bf16 v[40:43], v[176:179], v[104:107], v[40:43]\n\t"
        "ds_read_b128 v[140:143], %[at1] offset:22528\n\t"
        "v_mfma_f32_16x16x32_bf16 v[56:59], v[180:183], v[104:107], v[56:59]\n\t"
        "buffer_load_dwordx4 v[168:171], %[vf], s[44:47], s53 offen offset:2048 sc1\n\t"
        "v_mfma_f32_16x16x32_bf16 v[60:63], v[180:183], v[108:111], v[60:63]\n\t"
        "ds_read_b128 v[144:147], %[at1] offset:24576\n\t"
        "v_mfma_f32_16x16x32_bf16 v[44:47], v[176:179], v[108:111], v[44:47]\n\t"
        "v_mfma_f32_16x16x32_bf16 v[64:67], v[176:179], v[112:115], v[64:67]\n\t"
        "ds_read_b128 v[148:151], %[at1] offset:26624\n\t"
        "v_mfma_f32_16x16x32_bf16 v[80:83], v[180:183], v[112:115], v[80:83]\n\t"
        "ds_read_b128 v[152:155], %[at1] offset:28672\n\t"
        "buffer_load_dwordx4 %[vt3], s[40:43], s52 offen offset:3072 lds\n\t"
        "v_mfma_f32_16x16x32_bf16 v[84:87], v[180:183], v[116:119], v[84:87]\n\t"
        "v_mfma_f32_16x16x32_bf16 v[68:71], v[176:179], v[116:119], v[68:71]\n\t"
        "ds_read_b128 v[156:159], %[at1] offset:30720\n\t"
        "s_cmp_lg_u32 %[pre], 0\n\t"
        "s_cbranch_scc0 6f\n\t"
        "s_mov_b32 s62, 28672\n\t"
        "s_add_u32 m0, %[cM], 65536\n\t"
        "buffer_load_dwordx4 %[vf], s[44:47], s62 offen offset:0 sc1 lds\n\t"
        "buffer_load_dwordx4 %[vf], s[44:47], s62 offen offset:1024 sc1 lds\n\t"
        "buffer_load_dwordx4 %[vf], s[44:47], s62 offen offset:2048 sc1 lds\n\t"
        "buffer_load_dwordx4 %[vf], s[44:47], s62 offen offset:3072 sc1 lds\n\t"
        "6:\n\t"
        "v_mfma_f32_16x16x32_bf16 v[72:75], v[176:179], v[120:123], v[72:75]\n\t"
        "v_mfma_f32_16x16x32_bf16 v[88:91], v[180:183], v[120:123], v[88:91]\n\t"
        "buffer_load_dwordx4 v[172:175], %[vf], s[44:47], s53 offen offset:3072 sc1\n\t"
        "v_mfma_f32_16x16x32_bf16 v[92:95], v[180:183], v[124:127], v[92:95]\n\t"
        "s_add_u32 s52, s52, 128\n\t"
        "s_add_u32 s53, s53, 4096\n\t"
        "v_mfma_f32_16x16x32_bf16 v[76:79], v[176:179], v[124:127], v[76:79]\n\t"
        "s_and_b32 s52, s52, 4095\n\t"
        "s_and_b32 s53, s53, 0x1ffff\n\t"
        "s_waitcnt vmcnt(17) lgkmcnt(0)\n\t"
        "s_barrier\n\t"
        "s_add_u32 m0, %[cM], 16384\n\t"
        "v_mfma_f32_16x16x32_bf16 v[32:35], v[184:187], v[128:131], v[32:35]\n\t"
        "ds_read_b128 v[96:99], %[at0] offset:32768\n\t"
        "v_mfma_f32_16x16x32_bf16 v[48:51], v[188:191], v[128:131], v[48:51]\n\t"
        "ds_read_b128 v[100:103], %[at0] offset:34816\n\t"
        "buffer_load_dwordx4 %[vt0], s[40:43], s52 offen offset:0 lds\n\t"
        "v_mfma_f32_16x16x32_bf16 v[52:55], v[188:191], v[132:135], v[52:55]\n\t"
        "v_mfma_f32_16x16x32_bf16 v[36:39], v[184:187], v[132:135], v[36:39]\n\t"
        "ds_read_b128 v[104:107], %[at0] offset:36864\n\t"
        "v_mfma_f32_16x16x32_bf16 v[40:43], v[184:187], v[136:139], v[40:43]\n\t"
        "ds_read_b128 v[108:111], %[at0] offset:38912\n\t"
        "v_mfma_f32_16x16x32_bf16 v[56:59], v[188:191], v[136:139], v[56:59]\n\t"
        "buffer_load_dwordx4 v[176:179], %[vf], s[44:47], s53 offen offset:0 sc1\n\t"
        "v_mfma_f32_16x16x32_bf16 v[60:63], v[188:191], v[140:143], v[60:63]\n\t"
        "ds_read_b128 v[112:115], %[at0] offset:40960\n\t"
        "v_mfma_f32_16x16x32_bf16 v[44:47], v[184:187], v[140:143], v[44:47]\n\t"
        "v_mfma_f32_16x16x32_bf16 v[64:67], v[184:187], v[144:147], v[64:67]\n\t"
        "ds_read_b128 v[116:119], %[at0] offset:43008\n\t"
        "v_mfma_f32_16x16x32_bf16 v[80:83], v[188:191], v[144:147], v[80:83]\n\t"
        "ds_read_b128 v[120:123], %[at0] offset:45056\n\t"
        "buffer_load_dwordx4 %[vt1], s[40:43], s52 offen offset:1024 lds\n\t"
        "v_mfma_f32_16x16x32_bf16 v[84:87], v[188:191], v[148:151], v[84:87]\n\t"
        "v_mfma_f32_16x16x32_bf16 v[68:71], v[184:187], v[148:151], v[68:71]\n\t"
        "ds_read_b128 v[124:127], %[at0] offset:47104\n\t"
        "s_cmp_lg_u32 %[pre], 0\n\t"
        "s_cbranch_scc0 7f\n\t"
        "s_mov_b32 s62, 20480\n\t"
        "buffer_load_dwordx4 v[240:243], %[vf], s[44:47], s62 offen offset:0 sc1\n\t"
        "buffer_load_dwordx4 v[244:247], %[vf], s[44:47], s62 offen offset:1024 sc1\n\t"
        "buffer_load_dwordx4 v[248:251], %[vf], s[44:47], s62 offen offset:2048 sc1\n\t"
        "buffer_load_dwordx4 v[252:255], %[vf], s[44:47], s62 offen offset:3072 sc1\n\t"
        "7:\n\t"
        "v_mfma_f32_16x16x32_bf16 v[72:75], v[184:187], v[152:155], v[72:75]\n\t"
        "v_mfma_f32_16x16x32_bf16 v[88:91], v[188:191], v[152:155], v[88:91]\n\t"
        "buffer_load_dwordx4 v[180:183], %[vf], s[44:47], s53 offen offset:1024 sc1\n\t"
        "v_mfma_f32_16x16x32_bf16 v[92:95], v[188:191], v[156:159], v[92:95]\n\t"
        "v_mfma_f32_16x16x32_bf16 v[76:79], v[184:187], v[156:159], v[76:79]\n\t"
        "2:\n\t"
        "s_waitcnt vmcnt(24) lgkmcnt(0)\n\t"
        "v_mfma_f32_16x16x32_bf16 v[32:35], v[192:195], v[96:99], v[32:35]\n\t"
        "ds_read_b128 v[128:131], %[at1] offset:32768\n\t"
        "v_mfma_f32_16x16x32_bf16 v[48:51], v[196:199], v[96:99], v[48:51]\n\t"
        "ds_read_b128 v[132:135], %[at1] offset:34816\n\t"
        "buffer_load_dwordx4 %[vt2], s[40:43], s52 offen offset:2048 lds\n\t"
        "v_mfma_f32_16x16x32_bf16 v[52:55], v[196:199], v[100:103], v[52:55]\n\t"
        "v_cmp_ne_u64 vcc, %[tag], %[fw]\n\t"
        "v_mfma_f32_16x16x32_bf16 v[36:39], v[192:195], v[100:103], v[36:39]\n\t"
        "ds_read_b128 v[136:139], %[at1] offset:36864\n\t"
        "v_mfma_f32_16x16x32_bf16 v[40:43], v[192:195], v[104:107], v[40:43]\n\t"
        "ds_read_b128 v[140:143], %[at1] offset:38912\n\t"
        "s_cmp_eq_u64 vcc, 0\n\t"
        "s_cselect_b32 s55, 1, 0\n\t"
        "v_mfma_f32_16x16x32_bf16 v[56:59], v[196:199], v[104:107], v[56:59]\n\t"
        "buffer_load_dwordx4 v[184:187], %[vf], s[44:47], s53 offen offset:2048 sc1\n\t"
        "v_mfma_f32_16x16x32_bf16 v[60:63], v[196:199], v[108:111], v[60:63]\n\t"
        "ds_read_b128 v[144:147], %[at1] offset:40960\n\t"
        "buffer_load_dwordx2 %[fw], %[vfw], s[56:59], 0 offen sc1\n\t"
        "v_mfma_f32_16x16x32_bf16 v[44:47], v[192:195], v[108:111], v[44:47]\n\t"
        "v_mfma_f32_16x16x32_bf16 v[64:67], v[192:195], v[112:115], v[64:67]\n\t"
        "ds_read_b128 v[148:151], %[at1] offset:43008\n\t"
        "v_mfma_f32_16x16x32_bf16 v[80:83], v[196:199], v[112:115], v[80:83]\n\t"
        "ds_read_b128 v[152:155], %[at1] offset:45056\n\t"
        "buffer_load_dwordx4 %[vt3], s[40:43], s52 offen offset:3072 lds\n\t"
        "v_mfma_f32_16x16x32_bf16 v[84:87], v[196:199], v[116:119], v[84:87]\n\t"
        "v_mfma_f32_16x16x32_bf16 v[68:71], v[192:195], v[116:119], v[68:71]\n\t"
        "ds_read_b128 v[156:159], %[at1] offset:47104\n\t"
        "v_mfma_f32_16x16x32_bf16 v[72:75], v[192:195], v[120:123], v[72:75]\n\t"
        "v_mfma_f32_16x16x32_bf16 v[88:91], v[196:199], v[120:123], v[88:91]\n\t"
        "buffer_load_dwordx4 v[188:191], %[vf], s[44:47], s53 offen offset:3072 sc1\n\t"
        "v_mfma_f32_16x16x32_bf16 v[92:95], v[196:199], v[124:127], v[92:95]\n\t"
        "s_add_u32 s52, s52, 128\n\t"
        "s_add_u32 s53, s53, 4096\n\t"
        "v_mfma_f32_16x16x32_bf16 v[76:79], v[192:195], v[124:127], v[76:79]\n\t"
        "s_and_b32 s52, s52, 4095\n\t"
        "s_and_b32 s53, s53, 0x1ffff\n\t"
        "s_waitcnt vmcnt(18) lgkmcnt(0)\n\t"
        "s_barrier\n\t"
        "s_add_u32 m0, %[cM], 32768\n\t"
        "v_mfma_f32_16x16x32_bf16 v[32:35], v[200:203], v[128:131], v[32:35]\n\t"
        "ds_read_b128 v[96:99], %[at0] offset:49152\n\t"
        "v_mfma_f32_16x16x32_bf16 v[48:51], v[204:207], v[128:131], v[48:51]\n\t"
        "ds_read_b128 v[100:103], %[at0] offset:51200\n\t"
        "buffer_load_dwordx4 %[vt0], s[40:43], s52 offen offset:0 lds\n\t"
        "v_mfma_f32_16x16x32_bf16 v[52:55], v[204:207], v[132:135], v[52:55]\n\t"
        "v_mfma_f32_16x16x32_bf16 v[36:39], v[200:203], v[132:135], v[36:39]\n\t"
        "ds_read_b128 v[104:107], %[at0] offset:53248\n\t"
        "v_mfma_f32_16x16x32_bf16 v[40:43], v[200:203], v[136:139], v[40:43]\n\t"
        "ds_read_b128 v[108:111], %[at0] offset:55296\n\t"
        "v_mfma_f32_16x16x32_bf16 v[56:59], v[204:207], v[136:139], v[56:59]\n\t"
        "buffer_load_dwordx4 v[192:195], %[vf], s[44:47], s53 offen offset:0 sc1\n\t"
        "v_mfma_f32_16x16x32_bf16 v[60:63], v[204:207], v[140:143], v[60:63]\n\t"
        "ds_read_b128 v[112:115], %[at0] offset:57344\n\t"
        "v_mfma_f32_16x16x32_bf16 v[44:47], v[200:203], v[140:143], v[44:47]\n\t"
        "v_mfma_f32_16x16x32_bf16 v[64:67], v[200:203], v[144:147], v[64:67]\n\t"
        "ds_read_b128 v[116:119], %[at0] offset:59392\n\t"
        "v_mfma_f32_16x16x32_bf16 v[80:83], v[204:207], v[144:147], v[80:83]\n\t"
        "ds_read_b128 v[120:123], %[at0] offset:61440\n\t"
        "buffer_load_dwordx4 %[vt1], s[40:43], s52 offen offset:1024 lds\n\t"
        "v_mfma_f32_16x16x32_bf16 v[84:87], v[204:207], v[148:151], v[84:87]\n\t"
        "v_mfma_f32_16x16x32_bf16 v[68:71], v[200:203], v[148:151], v[68:71]\n\t"
        "ds_read_b128 v[124:127], %[at0] offset:63488\n\t"
        "v_mfma_f32_16x16x32_bf16 v[72:75], v[200:203], v[152:155], v[72:75]\n\t"
        "v_mfma_f32_16x16x32_bf16 v[88:91], v[204:207], v[152:155], v[88:91]\n\t"
        "buffer_load_dwordx4 v[196:199], %[vf], s[44:47], s53 offen offset:1024 sc1\n\t"
        "v_mfma_f32_16x16x32_bf16 v[92:95], v[204:207], v[156:159], v[92:95]\n\t"
        "v_mfma_f32_16x16x32_bf16 v[76:79], v[200:203], v[156:159], v[76:79]\n\t"
        "s_waitcnt vmcnt(25) lgkmcnt(0)\n\t"
        "v_mfma_f32_16x16x32_bf16 v[32:35], v[208:211], v[96:99], v[32:35]\n\t"
        "ds_read_b128 v[128:131], %[at1] offset:49152\n\t"
        "v_mfma_f32_16x16x32_bf16 v[48:51], v[212:215], v[96:99], v[48:51]\n\t"
        "ds_read_b128 v[132:135], %[at1] offset:51200\n\t"
        "buffer_load_dwordx4 %[vt2], s[40:43], s52 offen offset:2048 lds\n\t"
        "v_mfma_f32_16x16x32_bf16 v[52:55], v[212:215], v[100:103], v[52:55]\n\t"
        "v_mfma_f32_16x16x32_bf16 v[36:39], v[208:211], v[100:103], v[36:39]\n\t"
        "ds_read_b128 v[136:139], %[at1] offset:53248\n\t"
        "v_mfma_f32_16x16x32_bf16 v[40:43], v[208:211], v[104:107], v[40:43]\n\t"
        "ds_read_b128 v[140:143], %[at1] offset:55296\n\t"
        "v_mfma_f32_16x16x32_bf16 v[56:59], v[212:215], v[104:107], v[56:59]\n\t"
        "buffer_load_dwordx4 v[200:203], %[vf], s[44:47], s53 offen offset:2048 sc1\n\t"
        "v_mfma_f32_16x16x32_bf16 v[60:63], v[212:215], v[108:111], v[60:63]\n\t"
        "ds_read_b128 v[144:147], %[at1] offset:57344\n\t"
        "v_mfma_f32_16x16x32_bf16 v[44:47], v[208:211], v[108:111], v[44:47]\n\t"
        "v_mfma_f32_16x16x32_bf16 v[64:67], v[208:211], v[112:115], v[64:67]\n\t"
        "ds_read_b128 v[148:151], %[at1] offset:59392\n\t"
        "v_mfma_f32_16x16x32_bf16 v[80:83], v[212:215], v[112:115], v[80:83]\n\t"
        "ds_read_b128 v[152:155], %[at1] offset:61440\n\t"
        "buffer_load_dwordx4 %[vt3], s[40:43], s52 offen offset:3072 lds\n\t"
        "v_mfma_f32_16x16x32_bf16 v[84:87], v[212:215], v[116:119], v[84:87]\n\t"
        "v_mfma_f32_16x16x32_bf16 v[68:71], v[208:211], v[116:119], v[68:71]\n\t"
        "ds_read_b128 v[156:159], %[at1] offset:63488\n\t"
        "v_mfma_f32_16x16x32_bf16 v[72:75], v[208:211], v[120:123], v[72:75]\n\t"
        "v_mfma_f32_16x16x32_bf16 v[88:91], v[212:215], v[120:123], v[88:91]\n\t"
        "buffer_load_dwordx4 v[204:207], %[vf], s[44:47], s53 offen offset:3072 sc1\n\t"
        "v_mfma_f32_16x16x32_bf16 v[92:95], v[212:215], v[124:127], v[92:95]\n\t"
        "s_add_u32 s52, s52, 128\n\t"
        "s_add_u32 s53, s53, 4096\n\t"
        "v_mfma_f32_16x16x32_bf16 v[76:79], v[208:211], v[124:127], v[76:79]\n\t"
        "s_and_b32 s52, s52, 4095\n\t"
        "s_and_b32 s53, s53, 0x1ffff\n\t"
        "s_waitcnt vmcnt(18) lgkmcnt(0)\n\t"
        "s_barrier\n\t"
        "s_add_u32 m0, %[cM], 49152\n\t"
        "v_mfma_f32_16x16x32_bf16 v[32:35], v[216:219], v[128:131], v[32:35]\n\t"
        "ds_read_b128 v[96:99], %[at0] offset:0\n\t"
        "v_mfma_f32_16x16x32_bf16 v[48:51], v[220:223], v[128:131], v[48:51]\n\t"
        "ds_read_b128 v[100:103], %[at0] offset:2048\n\t"
        "buffer_load_dwordx4 %[vt0], s[40:43], s52 offen offset:0 lds\n\t"
        "v_mfma_f32_16x16x32_bf16 v[52:55], v[220:223], v[132:135], v[52:55]\n\t"
        "v_mfma_f32_16x16x32_bf16 v[36:39], v[216:219], v[132:135], v[36:39]\n\t"
        "ds_read_b128 v[104:107], %[at0] offset:4096\n\t"
        "v_mfma_f32_16x16x32_bf16 v[40:43], v[216:219], v[136:139], v[40:43]\n\t"
        "ds_read_b128 v[108:111], %[at0] offset:6144\n\t"
        "v_mfma_f32_16x16x32_bf16 v[56:59], v[220:223], v[136:139], v[56:59]\n\t"
        "buffer_load_dwordx4 v[208:211], %[vf], s[44:47], s53 offen offset:0 sc1\n\t"
        "v_mfma_f32_16x16x32_bf16 v[60:63], v[220:223], v[140:143], v[60:63]\n\t"
        "ds_read_b128 v[112:115], %[at0] offset:8192\n\t"
        "v_mfma_f32_16x16x32_bf16 v[44:47], v[216:219], v[140:143], v[44:47]\n\t"
        "v_mfma_f32_16x16x32_bf16 v[64:67], v[216:219], v[144:147], v[64:67]\n\t"
        "ds_read_b128 v[116:119], %[at0] offset:10240\n\t"
        "v_mfma_f32_16x16x32_bf16 v[80:83], v[220:223], v[144:147], v[80:83]\n\t"
        "ds_read_b128 v[120:123], %[at0] offset:12288\n\t"
        "buffer_load_dwordx4 %[vt1], s[40:43], s52 offen offset:1024 lds\n\t"
        "v_mfma_f32_16x16x32_bf16 v[84:87], v[220:223], v[148:151], v[84:87]\n\t"
        "v_mfma_f32_16x16x32_bf16 v[68:71], v[216:219], v[148:151], v[68:71]\n\t"
        "ds_read_b128 v[124:127], %[at0] offset:14336\n\t"
        "v_mfma_f32_16x16x32_bf16 v[72:75], v[216:219], v[152:155], v[72:75]\n\t"
        "v_mfma_f32_16x16x32_bf16 v[88:91], v[220:223], v[152:155], v[88:91]\n\t"
        "buffer_load_dwordx4 v[212:215], %[vf], s[44:47], s53 offen offset:1024 sc1\n\t"
        "v_mfma_f32_16x16x32_bf16 v[92:95], v[220:223], v[156:159], v[92:95]\n\t"
        "v_mfma_f32_16x16x32_bf16 v[76:79], v[216:219], v[156:159], v[76:79]\n\t"
        "s_add_u32 s54, s54, 1\n\t"
        "s_cmp_lt_u32 s54, 8\n\t"
        "s_cbranch_scc1 1b\n\t"
        "s_waitcnt lgkmcnt(0)\n\t"
        "s_barrier\n\t"
        "s_nop 7\n\t"
        : "+{v[32:47]}"(ay0), "+{v[48:63]}"(ay1), "+{v[64:79]}"(an0), "+{v[80:95]}"(an1), [fw]"=&v"(fw), "=&{v[160:175]}"(wf0), "=&{v[176:191]}"(wf1), "=&{v[192:207]}"(wf2), "=&{v[208:223]}"(wf3), "=&{v[224:239]}"(wf4), "=&{v[240:255]}"(wf5), [pre]"=&s"(pre)
        : [vt0]"v"(vt0), [vt1]"v"(vt1), [vt2]"v"(vt2), [vt3]"v"(vt3), [at0]"v"(at0), [at1]"v"(at1), [vf]"v"(vf), [cM]"s"(cM), [pws]"s"(pws), [pf]"s"(pf), [vfw]"v"(vfw), [rot]"s"(rot), [pfw]"s"(pfw), [tag]"s"(tag), [pwo]"s"(pfo + 8192)
        : "v96", "v97", "v98", "v99", "v100", "v101", "v102", "v103", "v104", "v105", "v106", "v107", "v108", "v109", "v110", "v111", "v112", "v113", "v114", "v115", "v116", "v117", "v118", "v119", "v120", "v121", "v122", "v123", "v124", "v125", "v126", "v127", "v128", "v129", "v130", "v131", "v132", "v133", "v134", "v135", "v136", "v137", "v138", "v139", "v140", "v141", "v142", "v143", "v144", "v145", "v146", "v147", "v148", "v149", "v150", "v151", "v152", "v153", "v154", "v155", "v156", "v157", "v158", "v159", "s40", "s41", "s42", "s43", "s44", "s45", "s46", "s47", "s48", "s49", "s50", "s51", "s52", "s53", "s54", "s55", "s56", "s57", "s58", "s59", "s60", "s61", "s62", "vcc", "scc", "memory");

        const unsigned xt = (t2 * 8 + eg) * 8 + w;
#pragma unroll
        for (int tt = 0; tt < 4; ++tt) __builtin_amdgcn_raw_buffer_store_b128(pack44(an0, 4 * tt, an1, 4 * tt), rs, WS_XB + (xt + p2 * 4) * 4096 + tt * 1024 + lane * 16, 0, WT);
        if (!pre) wait_flags(pfw + vfw, tagv);
        const char* po = ws + WS_O + tb * 131072;
        const char* pfl = ws + WS_FO + tb * 256; const char* pg = ws + WS_G + tb * 256;
        const char* pxi = ws + WS_XB + (xt + (1 - p2) * 4) * 4096;
        const char* pfx = ws + WS_FX + (xt + p2 * 4) * 8; const char* pfy = ws + WS_FX + (xt + (1 - p2) * 4) * 8;
        unsigned vfl = vfw; const unsigned vo = w * 4096 + vf, alds = lds0 + vf, cL = lds0;
        const float sgn = p2 ? -1.f : 1.f; const unsigned sob = tb * 64 * 2048 + eb * 64;
        f32x16 ao0, ao1; u32x16 pn; u64 fx;
    asm volatile(
        "s_mov_b64 s[44:45], %[pf]\n\t"
        "s_and_b32 s45, s45, 0xffff\n\t"
        "s_mov_b32 s46, 65536\n\t"
        "s_mov_b32 s47, 0x20000\n\t"
        "s_mov_b32 s52, 0\n\t"
        "s_add_u32 m0, %[cM], 65536\n\t"
        "s_cmp_lg_u32 %[pre], 0\n\t"
        "s_cbranch_scc1 4f\n\t"
        "s_mov_b32 s54, 8192\n\t"
        "buffer_load_dwordx4 v[160:163], %[vf], s[44:47], s54 offen offset:0 sc1\n\t"
        "s_mov_b32 s54, 8192\n\t"
        "buffer_load_dwordx4 v[164:167], %[vf], s[44:47], s54 offen offset:1024 sc1\n\t"
        "s_mov_b32 s54, 8192\n\t"
        "buffer_load_dwordx4 v[168:171], %[vf], s[44:47], s54 offen offset:2048 sc1\n\t"
        "s_mov_b32 s54, 8192\n\t"
        "buffer_load_dwordx4 v[172:175], %[vf], s[44:47], s54 offen offset:3072 sc1\n\t"
        "s_mov_b32 s54, 12288\n\t"
        "buffer_load_dwordx4 v[176:179], %[vf], s[44:47], s54 offen offset:0 sc1\n\t"
        "s_mov_b32 s54, 12288\n\t"
        "buffer_load_dwordx4 v[180:183], %[vf], s[44:47], s54 offen offset:1024 sc1\n\t"
        "s_mov_b32 s54, 12288\n\t"
        "buffer_load_dwordx4 v[184:187], %[vf], s[44:47], s54 offen offset:2048 sc1\n\t"
        "s_mov_b32 s54, 12288\n\t"
        "buffer_load_dwordx4 v[188:191], %[vf], s[44:47], s54 offen offset:3072 sc1\n\t"
        "s_mov_b32 s54, 16384\n\t"
        "buffer_load_dwordx4 v[192:195], %[vf], s[44:47], s54 offen offset:0 sc1\n\t"
        "s_mov_b32 s54, 16384\n\t"
        "buffer_load_dwordx4 v[196:199], %[vf], s[44:47], s54 offen offset:1024 sc1\n\t"
        "s_mov_b32 s54, 16384\n\t"
        "buffer_load_dwordx4 v[200:203], %[vf], s[44:47], s54 offen offset:2048 sc1\n\t"
        "s_mov_b32 s54, 16384\n\t"
        "buffer_load_dwordx4 v[204:207], %[vf], s[44:47], s54 offen offset:3072 sc1\n\t"
        "s_mov_b32 s54, 20480\n\t"
        "buffer_load_dwordx4 v[208:211], %[vf], s[44:47], s54 offen offset:0 sc1\n\t"
        "s_mov_b32 s54, 20480\n\t"
        "buffer_load_dwordx4 v[212:215], %[vf], s[44:47], s54 offen offset:1024 sc1\n\t"
        "s_mov_b32 s54, 24576\n\t"
        "buffer_load_dwordx4 v[224:227], %[vf], s[44:47], s54 offen offset:0 sc1\n\t"
        "s_mov_b32 s54, 24576\n\t"
        "buffer_load_dwordx4 v[228:231], %[vf], s[44:47], s54 offen offset:1024 sc1\n\t"
        "s_mov_b32 s54, 24576\n\t"
        "buffer_load_dwordx4 v[232:235], %[vf], s[44:47], s54 offen offset:2048 sc1\n\t"
        "s_mov_b32 s54, 24576\n\t"
        "buffer_load_dwordx4 v[236:239], %[vf], s[44:47], s54 offen offset:3072 sc1\n\t"
        "s_mov_b32 s54, 28672\n\t"
        "buffer_load_dwordx4 v[240:243], %[vf], s[44:47], s54 offen offset:0 sc1\n\t"
        "s_mov_b32 s54, 28672\n\t"
        "buffer_load_dwordx4 v[244:247], %[vf], s[44:47], s54 offen offset:1024 sc1\n\t"
        "s_mov_b32 s54, 28672\n\t"
        "buffer_load_dwordx4 v[248:251], %[vf], s[44:47], s54 offen offset:2048 sc1\n\t"
        "s_mov_b32 s54, 28672\n\t"
        "buffer_load_dwordx4 v[252:255], %[vf], s[44:47], s54 offen offset:3072 sc1\n\t"
        "s_mov_b32 s54, 36864\n\t"
        "buffer_load_dwordx4 %[vf], s[44:47], s54 offen offset:0 sc1 lds\n\t"
        "s_mov_b32 s54, 36864\n\t"
        "buffer_load_dwordx4 %[vf], s[44:47], s54 offen offset:1024 sc1 lds\n\t"
        "s_mov_b32 s54, 36864\n\t"
        "buffer_load_dwordx4 %[vf], s[44:47], s54 offen offset:2048 sc1 lds\n\t"
        "s_mov_b32 s54, 36864\n\t"
        "buffer_load_dwordx4 %[vf], s[44:47], s54 offen offset:3072 sc1 lds\n\t"
        "4:\n\t"
        "buffer_load_dwordx4 v[128:131], %[vf], s[44:47], s52 offen offset:0 sc1\n\t"
        "buffer_load_dwordx4 v[132:135], %[vf], s[44:47], s52 offen offset:1024 sc1\n\t"
        "buffer_load_dwordx4 v[136:139], %[vf], s[44:47], s52 offen offset:2048 sc1\n\t"
        "buffer_load_dwordx4 v[140:143], %[vf], s[44:47], s52 offen offset:3072 sc1\n\t"
        "s_add_u32 s52, s52, 4096\n\t"
        "buffer_load_dwordx4 v[144:147], %[vf], s[44:47], s52 offen offset:0 sc1\n\t"
        "buffer_load_dwordx4 v[148:151], %[vf], s[44:47], s52 offen offset:1024 sc1\n\t"
        "buffer_load_dwordx4 v[152:155], %[vf], s[44:47], s52 offen offset:2048 sc1\n\t"
        "buffer_load_dwordx4 v[156:159], %[vf], s[44:47], s52 offen offset:3072 sc1\n\t"
        "s_add_u32 s52, s52, 4096\n\t"
        "s_add_u32 s52, s52, 4096\n\t"
        "s_add_u32 s52, s52, 4096\n\t"
        "s_add_u32 s52, s52, 4096\n\t"
        "buffer_load_dwordx4 v[216:219], %[vf], s[44:47], s52 offen offset:2048 sc1\n\t"
        "buffer_load_dwordx4 v[220:223], %[vf], s[44:47], s52 offen offset:3072 sc1\n\t"
        "s_add_u32 s52, s52, 4096\n\t"
        "s_add_u32 s52, s52, 4096\n\t"
        "s_add_u32 s52, s52, 4096\n\t"
        "buffer_load_dwordx4 v[16:19], %[vf], s[44:47], s52 offen offset:0 sc1\n\t"
        "buffer_load_dwordx4 v[20:23], %[vf], s[44:47], s52 offen offset:1024 sc1\n\t"
        "buffer_load_dwordx4 v[24:27], %[vf], s[44:47], s52 offen offset:2048 sc1\n\t"
        "buffer_load_dwordx4 v[28:31], %[vf], s[44:47], s52 offen offset:3072 sc1\n\t"
        "s_add_u32 s52, s52, 4096\n\t"
        "s_mov_b64 s[40:41], %[po]\n\t"
        "s_and_b32 s41, s41, 0xffff\n\t"
        "s_mov_b32 s42, 131072\n\t"
        "s_mov_b32 s43, 0x20000\n\t"
        "s_mov_b64 s[48:49], %[px]\n\t"
        "s_and_b32 s49, s49, 0xffff\n\t"
        "s_mov_b32 s50, 4096\n\t"
        "s_mov_b32 s51, 0x20000\n\t"
        "s_mov_b32 s53, 0\n\t"
        "v_mov_b64 %[fx], %[tag]\n\t"
        "s_waitcnt vmcnt(14)\n\t"
        "s_mov_b64 exec, 1\n\t"
        "global_store_dwordx2 %[vf], %[fx], %[pfx] sc1\n\t"
        "s_mov_b64 exec, -1\n\t"
        "s_cmp_lg_u32 %[cM], %[cL]\n\t"
        "s_cbranch_scc1 5f\n\t"
        "3:\n\t"
        "global_load_dwordx2 v[112:113], %[vfl], %[pfl] sc1\n\t"
        "s_waitcnt vmcnt(0)\n\t"
        "v_cmp_ne_u64 vcc, %[tag], v[112:113]\n\t"
        "s_add_u32 s53, s53, 1\n\t"
        "s_cbranch_vccz 5f\n\t"
        "s_sleep 4\n\t"
        "s_cmp_lt_u32 s53, 0x100000\n\t"
        "s_cbranch_scc1 3b\n\t"
        "5:\n\t"
        "s_barrier\n\t"
        "s_add_u32 m0, %[cM], 0\n\t"
        "s_mov_b32 s54, 0\n\t"
        "buffer_load_dwordx4 %[vo], s[40:43], s54 offen offset:0 sc1 lds\n\t"
        "buffer_load_dwordx4 %[vo], s[40:43], s54 offen offset:1024 sc1 lds\n\t"
        "buffer_load_dwordx4 %[vo], s[40:43], s54 offen offset:2048 sc1 lds\n\t"
        "buffer_load_dwordx4 %[vo], s[40:43], s54 offen offset:3072 sc1 lds\n\t"
        "s_add_u32 m0, %[cM], 16384\n\t"
        "s_mov_b32 s54, 16384\n\t"
        "buffer_load_dwordx4 %[vo], s[40:43], s54 offen offset:0 sc1 lds\n\t"
        "buffer_load_dwordx4 %[vo], s[40:43], s54 offen offset:1024 sc1 lds\n\t"
        "buffer_load_dwordx4 %[vo], s[40:43], s54 offen offset:2048 sc1 lds\n\t"
        "buffer_load_dwordx4 %[vo], s[40:43], s54 offen offset:3072 sc1 lds\n\t"
        "s_add_u32 m0, %[cM], 32768\n\t"
        "s_mov_b32 s54, 32768\n\t"
        "buffer_load_dwordx4 %[vo], s[40:43], s54 offen offset:0 sc1 lds\n\t"
        "buffer_load_dwordx4 %[vo], s[40:43], s54 offen offset:1024 sc1 lds\n\t"
        "buffer_load_dwordx4 %[vo], s[40:43], s54 offen offset:2048 sc1 lds\n\t"
        "buffer_load_dwordx4 %[vo], s[40:43], s54 offen offset:3072 sc1 lds\n\t"
        "s_add_u32 m0, %[cM], 49152\n\t"
        "s_mov_b32 s54, 49152\n\t"
        "buffer_load_dwordx4 %[vo], s[40:43], s54 offen offset:0 sc1 lds\n\t"
        "buffer_load_dwordx4 %[vo], s[40:43], s54 offen offset:1024 sc1 lds\n\t"
        "buffer_load_dwordx4 %[vo], s[40:43], s54 offen offset:2048 sc1 lds\n\t"
        "buffer_load_dwordx4 %[vo], s[40:43], s54 offen offset:3072 sc1 lds\n\t"
        "s_add_u32 s55, %[cM], 65536\n\t"
        "v_add_u32 %[vfl], s55, %[vf]\n\t"
        "s_waitcnt vmcnt(12)\n\t"
        "s_barrier\n\t"
        "ds_read_b128 v[96:99], %[alds] offset:0\n\t"
        "ds_read_b128 v[100:103], %[alds] offset:4096\n\t"
        "ds_read_b128 v[104:107], %[alds] offset:8192\n\t"
        "ds_read_b128 v[108:111], %[alds] offset:12288\n\t"
        "s_waitcnt lgkmcnt(0)\n\t"
        "v_mfma_f32_16x16x32_bf16 v[64:67], v[128:131], v[96:99], 0\n\t"
        "ds_read_b128 v[112:115], %[alds] offset:1024\n\t"
        "v_mfma_f32_16x16x32_bf16 v[80:83], v[132:135], v[96:99], 0\n\t"
        "ds_read_b128 v[116:119], %[alds] offset:5120\n\t"
        "v_mfma_f32_16x16x32_bf16 v[68:71], v[128:131], v[100:103], 0\n\t"
        "ds_read_b128 v[120:123], %[alds] offset:9216\n\t"
        "v_mfma_f32_16x16x32_bf16 v[84:87], v[132:135], v[100:103], 0\n\t"
        "ds_read_b128 v[124:127], %[alds] offset:13312\n\t"
        "v_mfma_f32_16x16x32_bf16 v[72:75], v[128:131], v[104:107], 0\n\t"
        "v_mfma_f32_16x16x32_bf16 v[88:91], v[132:135], v[104:107], 0\n\t"
        "v_mfma_f32_16x16x32_bf16 v[76:79], v[128:131], v[108:111], 0\n\t"
        "v_mfma_f32_16x16x32_bf16 v[92:95], v[132:135], v[108:111], 0\n\t"
        "s_waitcnt lgkmcnt(0)\n\t"
        "v_mfma_f32_16x16x32_bf16 v[64:67], v[136:139], v[112:115], v[64:67]\n\t"
        "ds_read_b128 v[96:99], %[alds] offset:2048\n\t"
        "v_mfma_f32_16x16x32_bf16 v[80:83], v[140:143], v[112:115], v[80:83]\n\t"
        "ds_read_b128 v[100:103], %[alds] offset:6144\n\t"
        "v_mfma_f32_16x16x32_bf16 v[68:71], v[136:139], v[116:119], v[68:71]\n\t"
        "ds_read_b128 v[104:107], %[alds] offset:10240\n\t"
        "v_mfma_f32_16x16x32_bf16 v[84:87], v[140:143], v[116:119], v[84:87]\n\t"
        "ds_read_b128 v[108:111], %[alds] offset:14336\n\t"
        "v_mfma_f32_16x16x32_bf16 v[72:75], v[136:139], v[120:123], v[72:75]\n\t"
        "ds_read_b128 v[128:131], %[vfl] offset:0\n\t"
        "v_mfma_f32_16x16x32_bf16 v[88:91], v[140:143], v[120:123], v[88:91]\n\t"
        "v_mfma_f32_16x16x32_bf16 v[76:79], v[136:139], v[124:127], v[76:79]\n\t"
        "ds_read_b128 v[132:135], %[vfl] offset:1024\n\t"
        "v_mfma_f32_16x16x32_bf16 v[92:95], v[140:143], v[124:127], v[92:95]\n\t"
        "s_waitcnt lgkmcnt(0)\n\t"
        "v_mfma_f32_16x16x32_bf16 v[64:67], v[144:147], v[96:99], v[64:67]\n\t"
        "ds_read_b128 v[112:115], %[alds] offset:3072\n\t"
        "v_mfma_f32_16x16x32_bf16 v[80:83], v[148:151], v[96:99], v[80:83]\n\t"
        "ds_read_b128 v[116:119], %[alds] offset:7168\n\t"
        "v_mfma_f32_16x16x32_bf16 v[68:71], v[144:147], v[100:103], v[68:71]\n\t"
        "ds_read_b128 v[120:123], %[alds] offset:11264\n\t"
        "v_mfma_f32_16x16x32_bf16 v[84:87], v[148:151], v[100:103], v[84:87]\n\t"
        "ds_read_b128 v[124:127], %[alds] offset:15360\n\t"
        "v_mfma_f32_16x16x32_bf16 v[72:75], v[144:147], v[104:107], v[72:75]\n\t"
        "ds_read_b128 v[136:139], %[vfl] offset:2048\n\t"
        "v_mfma_f32_16x16x32_bf16 v[88:91], v[148:151], v[104:107], v[88:91]\n\t"
        "v_mfma_f32_16x16x32_bf16 v[76:79], v[144:147], v[108:111], v[76:79]\n\t"
        "ds_read_b128 v[140:143], %[vfl] offset:3072\n\t"
        "v_mfma_f32_16x16x32_bf16 v[92:95], v[148:151], v[108:111], v[92:95]\n\t"
        "s_waitcnt vmcnt(8) lgkmcnt(0)\n\t"
        "s_barrier\n\t"
        "s_add_u32 m0, %[cM], 0\n\t"
        "s_mov_b32 s54, 65536\n\t"
        "v_mfma_f32_16x16x32_bf16 v[64:67], v[152:155], v[112:115], v[64:67]\n\t"
        "ds_read_b128 v[96:99], %[alds] offset:16384\n\t"
        "v_mfma_f32_16x16x32_bf16 v[80:83], v[156:159], v[112:115], v[80:83]\n\t"
        "ds_read_b128 v[100:103], %[alds] offset:20480\n\t"
        "buffer_load_dwordx4 %[vo], s[40:43], s54 offen offset:0 sc1 lds\n\t"
        "v_mfma_f32_16x16x32_bf16 v[68:71], v[152:155], v[116:119], v[68:71]\n\t"
        "ds_read_b128 v[104:107], %[alds] offset:24576\n\t"
        "v_mfma_f32_16x16x32_bf16 v[84:87], v[156:159], v[116:119], v[84:87]\n\t"
        "ds_read_b128 v[108:111], %[alds] offset:28672\n\t"
        "buffer_load_dwordx4 %[vo], s[40:43], s54 offen offset:1024 sc1 lds\n\t"
        "v_mfma_f32_16x16x32_bf16 v[72:75], v[152:155], v[120:123], v[72:75]\n\t"
        "s_add_u32 s52, s52, 4096\n\t"
        "buffer_load_dwordx4 v[144:147], %[vf], s[44:47], s52 offen offset:0 sc1\n\t"
        "v_mfma_f32_16x16x32_bf16 v[88:91], v[156:159], v[120:123], v[88:91]\n\t"
        "buffer_load_dwordx4 %[vo], s[40:43], s54 offen offset:2048 sc1 lds\n\t"
        "v_mfma_f32_16x16x32_bf16 v[76:79], v[152:155], v[124:127], v[76:79]\n\t"
        "buffer_load_dwordx4 v[148:151], %[vf], s[44:47], s52 offen offset:1024 sc1\n\t"
        "v_mfma_f32_16x16x32_bf16 v[92:95], v[156:159], v[124:127], v[92:95]\n\t"
        "buffer_load_dwordx4 %[vo], s[40:43], s54 offen offset:3072 sc1 lds\n\t"
        "s_waitcnt lgkmcnt(0)\n\t"
        "v_mfma_f32_16x16x32_bf16 v[64:67], v[160:163], v[96:99], v[64:67]\n\t"
        "ds_read_b128 v[112:115], %[alds] offset:17408\n\t"
        "v_mfma_f32_16x16x32_bf16 v[80:83], v[164:167], v[96:99], v[80:83]\n\t"
        "ds_read_b128 v[116:119], %[alds] offset:21504\n\t"
        "v_mfma_f32_16x16x32_bf16 v[68:71], v[160:163], v[100:103], v[68:71]\n\t"
        "ds_read_b128 v[120:123], %[alds] offset:25600\n\t"
        "v_mfma_f32_16x16x32_bf16 v[84:87], v[164:167], v[100:103], v[84:87]\n\t"
        "ds_read_b128 v[124:127], %[alds] offset:29696\n\t"
        "v_mfma_f32_16x16x32_bf16 v[72:75], v[160:163], v[104:107], v[72:75]\n\t"
        "buffer_load_dwordx4 v[152:155], %[vf], s[44:47], s52 offen offset:2048 sc1\n\t"
        "v_mfma_f32_16x16x32_bf16 v[88:91], v[164:167], v[104:107], v[88:91]\n\t"
        "v_mfma_f32_16x16x32_bf16 v[76:79], v[160:163], v[108:111], v[76:79]\n\t"
        "buffer_load_dwordx4 v[156:159], %[vf], s[44:47], s52 offen offset:3072 sc1\n\t"
        "v_mfma_f32_16x16x32_bf16 v[92:95], v[164:167], v[108:111], v[92:95]\n\t"
        "s_waitcnt lgkmcnt(0)\n\t"
        "v_mfma_f32_16x16x32_bf16 v[64:67], v[168:171], v[112:115], v[64:67]\n\t"
        "ds_read_b128 v[96:99], %[alds] offset:18432\n\t"
        "v_mfma_f32_16x16x32_bf16 v[80:83], v[172:175], v[112:115], v[80:83]\n\t"
        "ds_read_b128 v[100:103], %[alds] offset:22528\n\t"
        "v_mfma_f32_16x16x32_bf16 v[68:71], v[168:171], v[116:119], v[68:71]\n\t"
        "ds_read_b128 v[104:107], %[alds] offset:26624\n\t"
        "v_mfma_f32_16x16x32_bf16 v[84:87], v[172:175], v[116:119], v[84:87]\n\t"
        "ds_read_b128 v[108:111], %[alds] offset:30720\n\t"
        "v_mfma_f32_16x16x32_bf16 v[72:75], v[168:171], v[120:123], v[72:75]\n\t"
        "s_add_u32 s52, s52, 4096\n\t"
        "buffer_load_dwordx4 v[160:163], %[vf], s[44:47], s52 offen offset:0 sc1\n\t"
        "v_mfma_f32_16x16x32_bf16 v[88:91], v[172:175], v[120:123], v[88:91]\n\t"
        "v_mfma_f32_16x16x32_bf16 v[76:79], v[168:171], v[124:127], v[76:79]\n\t"
        "buffer_load_dwordx4 v[164:167], %[vf], s[44:47], s52 offen offset:1024 sc1\n\t"
        "v_mfma_f32_16x16x32_bf16 v[92:95], v[172:175], v[124:127], v[92:95]\n\t"
        "s_waitcnt lgkmcnt(0)\n\t"
        "v_mfma_f32_16x16x32_bf16 v[64:67], v[176:179], v[96:99], v[64:67]\n\t"
        "ds_read_b128 v[112:115], %[alds] offset:19456\n\t"
        "v_mfma_f32_16x16x32_bf16 v[80:83], v[180:183], v[96:99], v[80:83]\n\t"
        "ds_read_b128 v[116:119], %[alds] offset:23552\n\t"
        "v_mfma_f32_16x16x32_bf16 v[68:71], v[176:179], v[100:103], v[68:71]\n\t"
        "ds_read_b128 v[120:123], %[alds] offset:27648\n\t"
        "v_mfma_f32_16x16x32_bf16 v[84:87], v[180:183], v[100:103], v[84:87]\n\t"
        "ds_read_b128 v[124:127], %[alds] offset:31744\n\t"
        "v_mfma_f32_16x16x32_bf16 v[72:75], v[176:179], v[104:107], v[72:75]\n\t"
        "buffer_load_dwordx4 v[168:171], %[vf], s[44:47], s52 offen offset:2048 sc1\n\t"
        "v_mfma_f32_16x16x32_bf16 v[88:91], v[180:183], v[104:107], v[88:91]\n\t"
        "v_mfma_f32_16x16x32_bf16 v[76:79], v[176:179], v[108:111], v[76:79]\n\t"
        "buffer_load_dwordx4 v[172:175], %[vf], s[44:47], s52 offen offset:3072 sc1\n\t"
        "v_mfma_f32_16x16x32_bf16 v[92:95], v[180:183], v[108:111], v[92:95]\n\t"
        "s_waitcnt vmcnt(16) lgkmcnt(0)\n\t"
        "s_barrier\n\t"
        "s_add_u32 m0, %[cM], 16384\n\t"
        "s_mov_b32 s54, 81920\n\t"
        "v_mfma_f32_16x16x32_bf16 v[64:67], v[184:187], v[112:115], v[64:67]\n\t"
        "ds_read_b128 v[96:99], %[alds] offset:32768\n\t"
        "v_mfma_f32_16x16x32_bf16 v[80:83], v[188:191], v[112:115], v[80:83]\n\t"
        "ds_read_b128 v[100:103], %[alds] offset:36864\n\t"
        "buffer_load_dwordx4 %[vo], s[40:43], s54 offen offset:0 sc1 lds\n\t"
        "v_mfma_f32_16x16x32_bf16 v[68:71], v[184:187], v[116:119], v[68:71]\n\t"
        "ds_read_b128 v[104:107], %[alds] offset:40960\n\t"
        "v_mfma_f32_16x16x32_bf16 v[84:87], v[188:191], v[116:119], v[84:87]\n\t"
        "ds_read_b128 v[108:111], %[alds] offset:45056\n\t"
        "buffer_load_dwordx4 %[vo], s[40:43], s54 offen offset:1024 sc1 lds\n\t"
        "v_mfma_f32_16x16x32_bf16 v[72:75], v[184:187], v[120:123], v[72:75]\n\t"
        "s_add_u32 s52, s52, 4096\n\t"
        "buffer_load_dwordx4 v[176:179], %[vf], s[44:47], s52 offen offset:0 sc1\n\t"
        "v_mfma_f32_16x16x32_bf16 v[88:91], v[188:191], v[120:123], v[88:91]\n\t"
        "buffer_load_dwordx4 %[vo], s[40:43], s54 offen offset:2048 sc1 lds\n\t"
        "v_mfma_f32_16x16x32_bf16 v[76:79], v[184:187], v[124:127], v[76:79]\n\t"
        "buffer_load_dwordx4 v[180:183], %[vf], s[44:47], s52 offen offset:1024 sc1\n\t"
        "v_mfma_f32_16x16x32_bf16 v[92:95], v[188:191], v[124:127], v[92:95]\n\t"
        "buffer_load_dwordx4 %[vo], s[40:43], s54 offen offset:3072 sc1 lds\n\t"
        "s_waitcnt lgkmcnt(0)\n\t"
        "v_mfma_f32_16x16x32_bf16 v[64:67], v[192:195], v[96:99], v[64:67]\n\t"
        "ds_read_b128 v[112:115], %[alds] offset:33792\n\t"
        "v_mfma_f32_16x16x32_bf16 v[80:83], v[196:199], v[96:99], v[80:83]\n\t"
        "ds_read_b128 v[116:119], %[alds] offset:37888\n\t"
        "v_mfma_f32_16x16x32_bf16 v[68:71], v[192:195], v[100:103], v[68:71]\n\t"
        "ds_read_b128 v[120:123], %[alds] offset:41984\n\t"
        "v_mfma_f32_16x16x32_bf16 v[84:87], v[196:199], v[100:103], v[84:87]\n\t"
        "ds_read_b128 v[124:127], %[alds] offset:46080\n\t"
        "v_mfma_f32_16x16x32_bf16 v[72:75], v[192:195], v[104:107], v[72:75]\n\t"
        "buffer_load_dwordx4 v[184:187], %[vf], s[44:47], s52 offen offset:2048 sc1\n\t"
        "v_mfma_f32_16x16x32_bf16 v[88:91], v[196:199], v[104:107], v[88:91]\n\t"
        "v_mfma_f32_16x16x32_bf16 v[76:79], v[192:195], v[108:111], v[76:79]\n\t"
        "buffer_load_dwordx4 v[188:191], %[vf], s[44:47], s52 offen offset:3072 sc1\n\t"
        "v_mfma_f32_16x16x32_bf16 v[92:95], v[196:199], v[108:111], v[92:95]\n\t"
        "s_waitcnt lgkmcnt(0)\n\t"
        "v_mfma_f32_16x16x32_bf16 v[64:67], v[200:203], v[112:115], v[64:67]\n\t"
        "ds_read_b128 v[96:99], %[alds] offset:34816\n\t"
        "v_mfma_f32_16x16x32_bf16 v[80:83], v[204:207], v[112:115], v[80:83]\n\t"
        "ds_read_b128 v[100:103], %[alds] offset:38912\n\t"
        "v_mfma_f32_16x16x32_bf16 v[68:71], v[200:203], v[116:119], v[68:71]\n\t"
        "ds_read_b128 v[104:107], %[alds] offset:43008\n\t"
        "v_mfma_f32_16x16x32_bf16 v[84:87], v[204:207], v[116:119], v[84:87]\n\t"
        "ds_read_b128 v[108:111], %[alds] offset:47104\n\t"
        "v_mfma_f32_16x16x32_bf16 v[72:75], v[200:203], v[120:123], v[72:75]\n\t"
        "s_add_u32 s52, s52, 4096\n\t"
        "buffer_load_dwordx4 v[192:195], %[vf], s[44:47], s52 offen offset:0 sc1\n\t"
        "v_mfma_f32_16x16x32_bf16 v[88:91], v[204:207], v[120:123], v[88:91]\n\t"
        "global_load_dwordx2 %[fx], %[vz], %[pfy] sc1\n\t"
        "v_mfma_f32_16x16x32_bf16 v[76:79], v[200:203], v[124:127], v[76:79]\n\t"
        "buffer_load_dwordx4 v[196:199], %[vf], s[44:47], s52 offen offset:1024 sc1\n\t"
        "v_mfma_f32_16x16x32_bf16 v[92:95], v[204:207], v[124:127], v[92:95]\n\t"
        "s_waitcnt lgkmcnt(0)\n\t"
        "v_mfma_f32_16x16x32_bf16 v[64:67], v[208:211], v[96:99], v[64:67]\n\t"
        "ds_read_b128 v[112:115], %[alds] offset:35840\n\t"
        "v_mfma_f32_16x16x32_bf16 v[80:83], v[212:215], v[96:99], v[80:83]\n\t"
        "ds_read_b128 v[116:119], %[alds] offset:39936\n\t"
        "v_mfma_f32_16x16x32_bf16 v[68:71], v[208:211], v[100:103], v[68:71]\n\t"
        "ds_read_b128 v[120:123], %[alds] offset:44032\n\t"
        "v_mfma_f32_16x16x32_bf16 v[84:87], v[212:215], v[100:103], v[84:87]\n\t"
        "ds_read_b128 v[124:127], %[alds] offset:48128\n\t"
        "v_mfma_f32_16x16x32_bf16 v[72:75], v[208:211], v[104:107], v[72:75]\n\t"
        "buffer_load_dwordx4 v[200:203], %[vf], s[44:47], s52 offen offset:2048 sc1\n\t"
        "v_mfma_f32_16x16x32_bf16 v[88:91], v[212:215], v[104:107], v[88:91]\n\t"
        "v_mfma_f32_16x16x32_bf16 v[76:79], v[208:211], v[108:111], v[76:79]\n\t"
        "buffer_load_dwordx4 v[204:207], %[vf], s[44:47], s52 offen offset:3072 sc1\n\t"
        "v_mfma_f32_16x16x32_bf16 v[92:95], v[212:215], v[108:111], v[92:95]\n\t"
        "s_waitcnt vmcnt(25) lgkmcnt(0)\n\t"
        "s_barrier\n\t"
        "s_add_u32 m0, %[cM], 32768\n\t"
        "s_mov_b32 s54, 98304\n\t"
        "v_mfma_f32_16x16x32_bf16 v[64:67], v[216:219], v[112:115], v[64:67]\n\t"
        "ds_read_b128 v[96:99], %[alds] offset:49152\n\t"
        "v_mfma_f32_16x16x32_bf16 v[80:83], v[220:223], v[112:115], v[80:83]\n\t"
        "ds_read_b128 v[100:103], %[alds] offset:53248\n\t"
        "buffer_load_dwordx4 %[vo], s[40:43], s54 offen offset:0 sc1 lds\n\t"
        "v_mfma_f32_16x16x32_bf16 v[68:71], v[216:219], v[116:119], v[68:71]\n\t"
        "ds_read_b128 v[104:107], %[alds] offset:57344\n\t"
        "v_mfma_f32_16x16x32_bf16 v[84:87], v[220:223], v[116:119], v[84:87]\n\t"
        "ds_read_b128 v[108:111], %[alds] offset:61440\n\t"
        "buffer_load_dwordx4 %[vo], s[40:43], s54 offen offset:1024 sc1 lds\n\t"
        "v_mfma_f32_16x16x32_bf16 v[72:75], v[216:219], v[120:123], v[72:75]\n\t"
        "s_add_u32 s52, s52, 4096\n\t"
        "buffer_load_dwordx4 v[208:211], %[vf], s[44:47], s52 offen offset:0 sc1\n\t"
        "v_mfma_f32_16x16x32_bf16 v[88:91], v[220:223], v[120:123], v[88:91]\n\t"
        "buffer_load_dwordx4 %[vo], s[40:43], s54 offen offset:2048 sc1 lds\n\t"
        "v_mfma_f32_16x16x32_bf16 v[76:79], v[216:219], v[124:127], v[76:79]\n\t"
        "buffer_load_dwordx4 v[212:215], %[vf], s[44:47], s52 offen offset:1024 sc1\n\t"
        "v_mfma_f32_16x16x32_bf16 v[92:95], v[220:223], v[124:127], v[92:95]\n\t"
        "buffer_load_dwordx4 %[vo], s[40:43], s54 offen offset:3072 sc1 lds\n\t"
        "s_waitcnt lgkmcnt(0)\n\t"
        "v_mfma_f32_16x16x32_bf16 v[64:67], v[224:227], v[96:99], v[64:67]\n\t"
        "ds_read_b128 v[112:115], %[alds] offset:50176\n\t"
        "v_mfma_f32_16x16x32_bf16 v[80:83], v[228:231], v[96:99], v[80:83]\n\t"
        "ds_read_b128 v[116:119], %[alds] offset:54272\n\t"
        "v_mfma_f32_16x16x32_bf16 v[68:71], v[224:227], v[100:103], v[68:71]\n\t"
        "ds_read_b128 v[120:123], %[alds] offset:58368\n\t"
        "v_mfma_f32_16x16x32_bf16 v[84:87], v[228:231], v[100:103], v[84:87]\n\t"
        "ds_read_b128 v[124:127], %[alds] offset:62464\n\t"
        "v_mfma_f32_16x16x32_bf16 v[72:75], v[224:227], v[104:107], v[72:75]\n\t"
        "buffer_load_dwordx4 v[216:219], %[vf], s[44:47], s52 offen offset:2048 sc1\n\t"
        "v_mfma_f32_16x16x32_bf16 v[88:91], v[228:231], v[104:107], v[88:91]\n\t"
        "v_mfma_f32_16x16x32_bf16 v[76:79], v[224:227], v[108:111], v[76:79]\n\t"
        "buffer_load_dwordx4 v[220:223], %[vf], s[44:47], s52 offen offset:3072 sc1\n\t"
        "v_mfma_f32_16x16x32_bf16 v[92:95], v[228:231], v[108:111], v[92:95]\n\t"
        "s_waitcnt lgkmcnt(0)\n\t"
        "v_mfma_f32_16x16x32_bf16 v[64:67], v[232:235], v[112:115], v[64:67]\n\t"
        "ds_read_b128 v[96:99], %[alds] offset:51200\n\t"
        "v_mfma_f32_16x16x32_bf16 v[80:83], v[236:239], v[112:115], v[80:83]\n\t"
        "ds_read_b128 v[100:103], %[alds] offset:55296\n\t"
        "v_mfma_f32_16x16x32_bf16 v[68:71], v[232:235], v[116:119], v[68:71]\n\t"
        "ds_read_b128 v[104:107], %[alds] offset:59392\n\t"
        "v_mfma_f32_16x16x32_bf16 v[84:87], v[236:239], v[116:119], v[84:87]\n\t"
        "ds_read_b128 v[108:111], %[alds] offset:63488\n\t"
        "v_mfma_f32_16x16x32_bf16 v[72:75], v[232:235], v[120:123], v[72:75]\n\t"
        "s_add_u32 s52, s52, 4096\n\t"
        "buffer_load_dwordx4 v[224:227], %[vf], s[44:47], s52 offen offset:0 sc1\n\t"
        "v_mfma_f32_16x16x32_bf16 v[88:91], v[236:239], v[120:123], v[88:91]\n\t"
        "v_mfma_f32_16x16x32_bf16 v[76:79], v[232:235], v[124:127], v[76:79]\n\t"
        "buffer_load_dwordx4 v[228:231], %[vf], s[44:47], s52 offen offset:1024 sc1\n\t"
        "v_mfma_f32_16x16x32_bf16 v[92:95], v[236:239], v[124:127], v[92:95]\n\t"
        "s_waitcnt lgkmcnt(0)\n\t"
        "v_mfma_f32_16x16x32_bf16 v[64:67], v[240:243], v[96:99], v[64:67]\n\t"
        "ds_read_b128 v[112:115], %[alds] offset:52224\n\t"
        "v_mfma_f32_16x16x32_bf16 v[80:83], v[244:247], v[96:99], v[80:83]\n\t"
        "ds_read_b128 v[116:119], %[alds] offset:56320\n\t"
        "v_mfma_f32_16x16x32_bf16 v[68:71], v[240:243], v[100:103], v[68:71]\n\t"
        "ds_read_b128 v[120:123], %[alds] offset:60416\n\t"
        "v_mfma_f32_16x16x32_bf16 v[84:87], v[244:247], v[100:103], v[84:87]\n\t"
        "ds_read_b128 v[124:127], %[alds] offset:64512\n\t"
        "v_mfma_f32_16x16x32_bf16 v[72:75], v[240:243], v[104:107], v[72:75]\n\t"
        "buffer_load_dwordx4 v[232:235], %[vf], s[44:47], s52 offen offset:2048 sc1\n\t"
        "v_mfma_f32_16x16x32_bf16 v[88:91], v[244:247], v[104:107], v[88:91]\n\t"
        "v_mfma_f32_16x16x32_bf16 v[76:79], v[240:243], v[108:111], v[76:79]\n\t"
        "buffer_load_dwordx4 v[236:239], %[vf], s[44:47], s52 offen offset:3072 sc1\n\t"
        "v_mfma_f32_16x16x32_bf16 v[92:95], v[244:247], v[108:111], v[92:95]\n\t"
        "s_waitcnt vmcnt(31) lgkmcnt(0)\n\t"
        "s_barrier\n\t"
        "s_add_u32 m0, %[cM], 49152\n\t"
        "s_mov_b32 s54, 114688\n\t"
        "v_mfma_f32_16x16x32_bf16 v[64:67], v[248:251], v[112:115], v[64:67]\n\t"
        "ds_read_b128 v[96:99], %[alds] offset:0\n\t"
        "v_mfma_f32_16x16x32_bf16 v[80:83], v[252:255], v[112:115], v[80:83]\n\t"
        "ds_read_b128 v[100:103], %[alds] offset:4096\n\t"
        "buffer_load_dwordx4 %[vo], s[40:43], s54 offen offset:0 sc1 lds\n\t"
        "v_mfma_f32_16x16x32_bf16 v[68:71], v[248:251], v[116:119], v[68:71]\n\t"
        "ds_read_b128 v[104:107], %[alds] offset:8192\n\t"
        "v_mfma_f32_16x16x32_bf16 v[84:87], v[252:255], v[116:119], v[84:87]\n\t"
        "ds_read_b128 v[108:111], %[alds] offset:12288\n\t"
        "buffer_load_dwordx4 %[vo], s[40:43], s54 offen offset:1024 sc1 lds\n\t"
        "v_mfma_f32_16x16x32_bf16 v[72:75], v[248:251], v[120:123], v[72:75]\n\t"
        "v_mfma_f32_16x16x32_bf16 v[88:91], v[252:255], v[120:123], v[88:91]\n\t"
        "buffer_load_dwordx4 %[vo], s[40:43], s54 offen offset:2048 sc1 lds\n\t"
        "v_mfma_f32_16x16x32_bf16 v[76:79], v[248:251], v[124:127], v[76:79]\n\t"
        "v_mfma_f32_16x16x32_bf16 v[92:95], v[252:255], v[124:127], v[92:95]\n\t"
        "buffer_load_dwordx4 %[vo], s[40:43], s54 offen offset:3072 sc1 lds\n\t"
        "s_waitcnt lgkmcnt(0)\n\t"
        "s_waitcnt vmcnt(19)\n\t"
        "6:\n\t"
        "v_cmp_ne_u64 vcc, %[tag], %[fx]\n\t"
        "s_add_u32 s53, s53, 1\n\t"
        "s_cbranch_vccz 7f\n\t"
        "global_load_dwordx2 %[fx], %[vz], %[pfy] sc1\n\t"
        "s_waitcnt vmcnt(0)\n\t"
        "s_cmp_lt_u32 s53, 0x200000\n\t"
        "s_cbranch_scc1 6b\n\t"
        "7:\n\t"
        "v_mfma_f32_16x16x32_bf16 v[64:67], v[16:19], v[96:99], v[64:67]\n\t"
        "ds_read_b128 v[112:115], %[alds] offset:1024\n\t"
        "v_mfma_f32_16x16x32_bf16 v[80:83], v[20:23], v[96:99], v[80:83]\n\t"
        "ds_read_b128 v[116:119], %[alds] offset:5120\n\t"
        "v_mfma_f32_16x16x32_bf16 v[68:71], v[16:19], v[100:103], v[68:71]\n\t"
        "ds_read_b128 v[120:123], %[alds] offset:9216\n\t"
        "v_mfma_f32_16x16x32_bf16 v[84:87], v[20:23], v[100:103], v[84:87]\n\t"
        "ds_read_b128 v[124:127], %[alds] offset:13312\n\t"
        "v_mfma_f32_16x16x32_bf16 v[72:75], v[16:19], v[104:107], v[72:75]\n\t"
        "buffer_load_dwordx4 v[240:243], %[vf], s[48:51], 0 offen offset:0 sc1\n\t"
        "v_mfma_f32_16x16x32_bf16 v[88:91], v[20:23], v[104:107], v[88:91]\n\t"
        "buffer_load_dwordx4 v[244:247], %[vf], s[48:51], 0 offen offset:1024 sc1\n\t"
        "v_mfma_f32_16x16x32_bf16 v[76:79], v[16:19], v[108:111], v[76:79]\n\t"
        "buffer_load_dwordx4 v[248:251], %[vf], s[48:51], 0 offen offset:2048 sc1\n\t"
        "v_mfma_f32_16x16x32_bf16 v[92:95], v[20:23], v[108:111], v[92:95]\n\t"
        "buffer_load_dwordx4 v[252:255], %[vf], s[48:51], 0 offen offset:3072 sc1\n\t"
        "s_waitcnt lgkmcnt(0)\n\t"
        "v_mfma_f32_16x16x32_bf16 v[64:67], v[24:27], v[112:115], v[64:67]\n\t"
        "ds_read_b128 v[96:99], %[alds] offset:2048\n\t"
        "v_mfma_f32_16x16x32_bf16 v[80:83], v[28:31], v[112:115], v[80:83]\n\t"
        "ds_read_b128 v[100:103], %[alds] offset:6144\n\t"
        "v_and_b32 v16, 0xf0, %[vf]\n\t"
        "global_load_dwordx4 v[16:19], v16, %[pg] sc1\n\t"
        "v_mfma_f32_16x16x32_bf16 v[68:71], v[24:27], v[116:119], v[68:71]\n\t"
        "ds_read_b128 v[104:107], %[alds] offset:10240\n\t"
        "v_mfma_f32_16x16x32_bf16 v[84:87], v[28:31], v[116:119], v[84:87]\n\t"
        "ds_read_b128 v[108:111], %[alds] offset:14336\n\t"
        "v_mfma_f32_16x16x32_bf16 v[72:75], v[24:27], v[120:123], v[72:75]\n\t"
        "v_mfma_f32_16x16x32_bf16 v[88:91], v[28:31], v[120:123], v[88:91]\n\t"
        "v_mfma_f32_16x16x32_bf16 v[76:79], v[24:27], v[124:127], v[76:79]\n\t"
        "v_mfma_f32_16x16x32_bf16 v[92:95], v[28:31], v[124:127], v[92:95]\n\t"
        "s_waitcnt lgkmcnt(0)\n\t"
        "v_mfma_f32_16x16x32_bf16 v[64:67], v[128:131], v[96:99], v[64:67]\n\t"
        "ds_read_b128 v[112:115], %[alds] offset:3072\n\t"
        "v_mfma_f32_16x16x32_bf16 v[80:83], v[132:135], v[96:99], v[80:83]\n\t"
        "ds_read_b128 v[116:119], %[alds] offset:7168\n\t"
        "v_mfma_f32_16x16x32_bf16 v[68:71], v[128:131], v[100:103], v[68:71]\n\t"
        "ds_read_b128 v[120:123], %[alds] offset:11264\n\t"
        "v_mfma_f32_16x16x32_bf16 v[84:87], v[132:135], v[100:103], v[84:87]\n\t"
        "ds_read_b128 v[124:127], %[alds] offset:15360\n\t"
        "v_mfma_f32_16x16x32_bf16 v[72:75], v[128:131], v[104:107], v[72:75]\n\t"
        "v_mfma_f32_16x16x32_bf16 v[88:91], v[132:135], v[104:107], v[88:91]\n\t"
        "v_mfma_f32_16x16x32_bf16 v[76:79], v[128:131], v[108:111], v[76:79]\n\t"
        "v_mfma_f32_16x16x32_bf16 v[92:95], v[132:135], v[108:111], v[92:95]\n\t"
        "s_waitcnt vmcnt(28) lgkmcnt(0)\n\t"
        "s_barrier\n\t"
        "v_mfma_f32_16x16x32_bf16 v[64:67], v[136:139], v[112:115], v[64:67]\n\t"
        "ds_read_b128 v[96:99], %[alds] offset:16384\n\t"
        "v_mfma_f32_16x16x32_bf16 v[80:83], v[140:143], v[112:115], v[80:83]\n\t"
        "ds_read_b128 v[100:103], %[alds] offset:20480\n\t"
        "v_mfma_f32_16x16x32_bf16 v[68:71], v[136:139], v[116:119], v[68:71]\n\t"
        "ds_read_b128 v[104:107], %[alds] offset:24576\n\t"
        "v_mfma_f32_16x16x32_bf16 v[84:87], v[140:143], v[116:119], v[84:87]\n\t"
        "ds_read_b128 v[108:111], %[alds] offset:28672\n\t"
        "v_mfma_f32_16x16x32_bf16 v[72:75], v[136:139], v[120:123], v[72:75]\n\t"
        "v_mfma_f32_16x16x32_bf16 v[88:91], v[140:143], v[120:123], v[88:91]\n\t"
        "v_mfma_f32_16x16x32_bf16 v[76:79], v[136:139], v[124:127], v[76:79]\n\t"
        "v_mfma_f32_16x16x32_bf16 v[92:95], v[140:143], v[124:127], v[92:95]\n\t"
        "s_waitcnt vmcnt(41) lgkmcnt(0)\n\t"
        "v_mfma_f32_16x16x32_bf16 v[64:67], v[144:147], v[96:99], v[64:67]\n\t"
        "ds_read_b128 v[112:115], %[alds] offset:17408\n\t"
        "v_mfma_f32_16x16x32_bf16 v[80:83], v[148:151], v[96:99], v[80:83]\n\t"
        "ds_read_b128 v[116:119], %[alds] offset:21504\n\t"
        "v_mfma_f32_16x16x32_bf16 v[68:71], v[144:147], v[100:103], v[68:71]\n\t"
        "ds_read_b128 v[120:123], %[alds] offset:25600\n\t"
        "v_mfma_f32_16x16x32_bf16 v[84:87], v[148:151], v[100:103], v[84:87]\n\t"
        "ds_read_b128 v[124:127], %[alds] offset:29696\n\t"
        "v_mfma_f32_16x16x32_bf16 v[72:75], v[144:147], v[104:107], v[72:75]\n\t"
        "v_mfma_f32_16x16x32_bf16 v[88:91], v[148:151], v[104:107], v[88:91]\n\t"
        "v_mfma_f32_16x16x32_bf16 v[76:79], v[144:147], v[108:111], v[76:79]\n\t"
        "v_mfma_f32_16x16x32_bf16 v[92:95], v[148:151], v[108:111], v[92:95]\n\t"
        "s_waitcnt vmcnt(38) lgkmcnt(0)\n\t"
        "v_mfma_f32_16x16x32_bf16 v[64:67], v[152:155], v[112:115], v[64:67]\n\t"
        "ds_read_b128 v[96:99], %[alds] offset:18432\n\t"
        "v_mfma_f32_16x16x32_bf16 v[80:83], v[156:159], v[112:115], v[80:83]\n\t"
        "ds_read_b128 v[100:103], %[alds] offset:22528\n\t"
        "v_mfma_f32_16x16x32_bf16 v[68:71], v[152:155], v[116:119], v[68:71]\n\t"
        "ds_read_b128 v[104:107], %[alds] offset:26624\n\t"
        "v_mfma_f32_16x16x32_bf16 v[84:87], v[156:159], v[116:119], v[84:87]\n\t"
        "ds_read_b128 v[108:111], %[alds] offset:30720\n\t"
        "v_mfma_f32_16x16x32_bf16 v[72:75], v[152:155], v[120:123], v[72:75]\n\t"
        "v_mfma_f32_16x16x32_bf16 v[88:91], v[156:159], v[120:123], v[88:91]\n\t"
        "v_mfma_f32_16x16x32_bf16 v[76:79], v[152:155], v[124:127], v[76:79]\n\t"
        "v_mfma_f32_16x16x32_bf16 v[92:95], v[156:159], v[124:127], v[92:95]\n\t"
        "s_waitcnt vmcnt(36) lgkmcnt(0)\n\t"
        "v_mfma_f32_16x16x32_bf16 v[64:67], v[160:163], v[96:99], v[64:67]\n\t"
        "ds_read_b128 v[112:115], %[alds] offset:19456\n\t"
        "v_mfma_f32_16x16x32_bf16 v[80:83], v[164:167], v[96:99], v[80:83]\n\t"
        "ds_read_b128 v[116:119], %[alds] offset:23552\n\t"
        "v_mfma_f32_16x16x32_bf16 v[68:71], v[160:163], v[100:103], v[68:71]\n\t"
        "ds_read_b128 v[120:123], %[alds] offset:27648\n\t"
        "v_mfma_f32_16x16x32_bf16 v[84:87], v[164:167], v[100:103], v[84:87]\n\t"
        "ds_read_b128 v[124:127], %[alds] offset:31744\n\t"
        "v_mfma_f32_16x16x32_bf16 v[72:75], v[160:163], v[104:107], v[72:75]\n\t"
        "v_mfma_f32_16x16x32_bf16 v[88:91], v[164:167], v[104:107], v[88:91]\n\t"
        "v_mfma_f32_16x16x32_bf16 v[76:79], v[160:163], v[108:111], v[76:79]\n\t"
        "v_mfma_f32_16x16x32_bf16 v[92:95], v[164:167], v[108:111], v[92:95]\n\t"
        "s_waitcnt vmcnt(15) lgkmcnt(0)\n\t"
        "s_barrier\n\t"
        "v_mfma_f32_16x16x32_bf16 v[64:67], v[168:171], v[112:115], v[64:67]\n\t"
        "ds_read_b128 v[96:99], %[alds] offset:32768\n\t"
        "v_mfma_f32_16x16x32_bf16 v[80:83], v[172:175], v[112:115], v[80:83]\n\t"
        "ds_read_b128 v[100:103], %[alds] offset:36864\n\t"
        "v_mfma_f32_16x16x32_bf16 v[68:71], v[168:171], v[116:119], v[68:71]\n\t"
        "ds_read_b128 v[104:107], %[alds] offset:40960\n\t"
        "v_mfma_f32_16x16x32_bf16 v[84:87], v[172:175], v[116:119], v[84:87]\n\t"
        "ds_read_b128 v[108:111], %[alds] offset:45056\n\t"
        "v_mfma_f32_16x16x32_bf16 v[72:75], v[168:171], v[120:123], v[72:75]\n\t"
        "v_mfma_f32_16x16x32_bf16 v[88:91], v[172:175], v[120:123], v[88:91]\n\t"
        "v_mfma_f32_16x16x32_bf16 v[76:79], v[168:171], v[124:127], v[76:79]\n\t"
        "v_mfma_f32_16x16x32_bf16 v[92:95], v[172:175], v[124:127], v[92:95]\n\t"
        "s_waitcnt vmcnt(29) lgkmcnt(0)\n\t"
        "v_mfma_f32_16x16x32_bf16 v[64:67], v[176:179], v[96:99], v[64:67]\n\t"
        "ds_read_b128 v[112:115], %[alds] offset:33792\n\t"
        "v_mfma_f32_16x16x32_bf16 v[80:83], v[180:183], v[96:99], v[80:83]\n\t"
        "ds_read_b128 v[116:119], %[alds] offset:37888\n\t"
        "v_mfma_f32_16x16x32_bf16 v[68:71], v[176:179], v[100:103], v[68:71]\n\t"
        "ds_read_b128 v[120:123], %[alds] offset:41984\n\t"
        "v_mfma_f32_16x16x32_bf16 v[84:87], v[180:183], v[100:103], v[84:87]\n\t"
        "ds_read_b128 v[124:127], %[alds] offset:46080\n\t"
        "v_mfma_f32_16x16x32_bf16 v[72:75], v[176:179], v[104:107], v[72:75]\n\t"
        "v_mfma_f32_16x16x32_bf16 v[88:91], v[180:183], v[104:107], v[88:91]\n\t"
        "v_mfma_f32_16x16x32_bf16 v[76:79], v[176:179], v[108:111], v[76:79]\n\t"
        "v_mfma_f32_16x16x32_bf16 v[92:95], v[180:183], v[108:111], v[92:95]\n\t"
        "s_waitcnt vmcnt(26) lgkmcnt(0)\n\t"
        "s_waitcnt vmcnt(0)\n\t"
        "v_mfma_f32_16x16x32_bf16 v[64:67], v[184:187], v[112:115], v[64:67]\n\t"
        "ds_read_b128 v[96:99], %[alds] offset:34816\n\t"
        "v_and_b32 v20, 0xf0, %[vf]\n\t"
        "v_lshrrev_b32 v21, 8, %[vf]\n\t"
        "v_lshlrev_b32 v20, 7, v20\n\t"
        "v_mfma_f32_16x16x32_bf16 v[80:83], v[188:191], v[112:115], v[80:83]\n\t"
        "ds_read_b128 v[100:103], %[alds] offset:38912\n\t"
        "v_lshl_add_u32 v20, v21, 4, v20\n\t"
        "v_add_u32 v20, %[sob], v20\n\t"
        "v_sub_f32 v28, 1.0, v16\n\t"
        "v_mfma_f32_16x16x32_bf16 v[68:71], v[184:187], v[116:119], v[68:71]\n\t"
        "ds_read_b128 v[104:107], %[alds] offset:43008\n\t"
        "v_mul_f32 v24, %[sgn], v16\n\t"
        "v_cvt_pk_bf16_f32 v28, v28, v28\n\t"
        "v_lshlrev_b32 v21, 16, v240\n\t"
        "v_mfma_f32_16x16x32_bf16 v[84:87], v[188:191], v[116:119], v[84:87]\n\t"
        "ds_read_b128 v[108:111], %[alds] offset:47104\n\t"
        "v_and_b32 v22, 0xffff0000, v240\n\t"
        "v_sub_f32 v32, v32, v21\n\t"
        "v_sub_f32 v33, v33, v22\n\t"
        "v_mfma_f32_16x16x32_bf16 v[72:75], v[184:187], v[120:123], v[72:75]\n\t"
        "v_mul_f32 v32, v24, v32\n\t"
        "v_mul_f32 v33, v24, v33\n\t"
        "v_lshlrev_b32 v21, 16, v241\n\t"
        "v_mfma_f32_16x16x32_bf16 v[88:91], v[188:191], v[120:123], v[88:91]\n\t"
        "v_and_b32 v22, 0xffff0000, v241\n\t"
        "v_sub_f32 v34, v34, v21\n\t"
        "v_mfma_f32_16x16x32_bf16 v[76:79], v[184:187], v[124:127], v[76:79]\n\t"
        "v_sub_f32 v35, v35, v22\n\t"
        "v_mul_f32 v34, v24, v34\n\t"
        "v_mfma_f32_16x16x32_bf16 v[92:95], v[188:191], v[124:127], v[92:95]\n\t"
        "v_mul_f32 v35, v24, v35\n\t"
        "v_lshlrev_b32 v21, 16, v242\n\t"
        "s_waitcnt vmcnt(23) lgkmcnt(0)\n\t"
        "v_mfma_f32_16x16x32_bf16 v[64:67], v[192:195], v[96:99], v[64:67]\n\t"
        "ds_read_b128 v[112:115], %[alds] offset:35840\n\t"
        "v_and_b32 v22, 0xffff0000, v242\n\t"
        "v_sub_f32 v48, v48, v21\n\t"
        "v_mfma_f32_16x16x32_bf16 v[80:83], v[196:199], v[96:99], v[80:83]\n\t"
        "ds_read_b128 v[116:119], %[alds] offset:39936\n\t"
        "v_sub_f32 v49, v49, v22\n\t"
        "v_mul_f32 v48, v24, v48\n\t"
        "v_mfma_f32_16x16x32_bf16 v[68:71], v[192:195], v[100:103], v[68:71]\n\t"
        "ds_read_b128 v[120:123], %[alds] offset:44032\n\t"
        "v_mul_f32 v49, v24, v49\n\t"
        "v_lshlrev_b32 v21, 16, v243\n\t"
        "v_mfma_f32_16x16x32_bf16 v[84:87], v[196:199], v[100:103], v[84:87]\n\t"
        "ds_read_b128 v[124:127], %[alds] offset:48128\n\t"
        "v_and_b32 v22, 0xffff0000, v243\n\t"
        "v_sub_f32 v50, v50, v21\n\t"
        "v_mfma_f32_16x16x32_bf16 v[72:75], v[192:195], v[104:107], v[72:75]\n\t"
        "v_sub_f32 v51, v51, v22\n\t"
        "v_mul_f32 v50, v24, v50\n\t"
        "v_mfma_f32_16x16x32_bf16 v[88:91], v[196:199], v[104:107], v[88:91]\n\t"
        "v_mul_f32 v51, v24, v51\n\t"
        "v_lshlrev_b32 v28, 16, v28\n\t"
        "v_mfma_f32_16x16x32_bf16 v[76:79], v[192:195], v[108:111], v[76:79]\n\t"
        "v_sub_f32 v29, 1.0, v17\n\t"
        "v_mul_f32 v25, %[sgn], v17\n\t"
        "v_mfma_f32_16x16x32_bf16 v[92:95], v[196:199], v[108:111], v[92:95]\n\t"
        "v_cvt_pk_bf16_f32 v29, v29, v29\n\t"
        "v_lshlrev_b32 v21, 16, v244\n\t"
        "s_waitcnt vmcnt(5) lgkmcnt(0)\n\t"
        "s_barrier\n\t"
        "v_mfma_f32_16x16x32_bf16 v[64:67], v[200:203], v[112:115], v[64:67]\n\t"
        "ds_read_b128 v[96:99], %[alds] offset:49152\n\t"
        "v_and_b32 v22, 0xffff0000, v244\n\t"
        "v_sub_f32 v36, v36, v21\n\t"
        "v_mfma_f32_16x16x32_bf16 v[80:83], v[204:207], v[112:115], v[80:83]\n\t"
        "ds_read_b128 v[100:103], %[alds] offset:53248\n\t"
        "v_sub_f32 v37, v37, v22\n\t"
        "v_mul_f32 v36, v25, v36\n\t"
        "v_mfma_f32_16x16x32_bf16 v[68:71], v[200:203], v[116:119], v[68:71]\n\t"
        "ds_read_b128 v[104:107], %[alds] offset:57344\n\t"
        "v_mul_f32 v37, v25, v37\n\t"
        "v_lshlrev_b32 v21, 16, v245\n\t"
        "v_mfma_f32_16x16x32_bf16 v[84:87], v[204:207], v[116:119], v[84:87]\n\t"
        "ds_read_b128 v[108:111], %[alds] offset:61440\n\t"
        "v_and_b32 v22, 0xffff0000, v245\n\t"
        "v_sub_f32 v38, v38, v21\n\t"
        "v_mfma_f32_16x16x32_bf16 v[72:75], v[200:203], v[120:123], v[72:75]\n\t"
        "v_sub_f32 v39, v39, v22\n\t"
        "v_mul_f32 v38, v25, v38\n\t"
        "v_mfma_f32_16x16x32_bf16 v[88:91], v[204:207], v[120:123], v[88:91]\n\t"
        "v_mul_f32 v39, v25, v39\n\t"
        "v_lshlrev_b32 v21, 16, v246\n\t"
        "v_mfma_f32_16x16x32_bf16 v[76:79], v[200:203], v[124:127], v[76:79]\n\t"
        "v_and_b32 v22, 0xffff0000, v246\n\t"
        "v_sub_f32 v52, v52, v21\n\t"
        "v_mfma_f32_16x16x32_bf16 v[92:95], v[204:207], v[124:127], v[92:95]\n\t"
        "v_sub_f32 v53, v53, v22\n\t"
        "v_mul_f32 v52, v25, v52\n\t"
        "s_waitcnt vmcnt(16) lgkmcnt(0)\n\t"
        "v_mfma_f32_16x16x32_bf16 v[64:67], v[208:211], v[96:99], v[64:67]\n\t"
        "ds_read_b128 v[112:115], %[alds] offset:50176\n\t"
        "v_mul_f32 v53, v25, v53\n\t"
        "v_lshlrev_b32 v21, 16, v247\n\t"
        "v_mfma_f32_16x16x32_bf16 v[80:83], v[212:215], v[96:99], v[80:83]\n\t"
        "ds_read_b128 v[116:119], %[alds] offset:54272\n\t"
        "v_and_b32 v22, 0xffff0000, v247\n\t"
        "v_sub_f32 v54, v54, v21\n\t"
        "v_mfma_f32_16x16x32_bf16 v[68:71], v[208:211], v[100:103], v[68:71]\n\t"
        "ds_read_b128 v[120:123], %[alds] offset:58368\n\t"
        "v_sub_f32 v55, v55, v22\n\t"
        "v_mul_f32 v54, v25, v54\n\t"
        "v_mfma_f32_16x16x32_bf16 v[84:87], v[212:215], v[100:103], v[84:87]\n\t"
        "ds_read_b128 v[124:127], %[alds] offset:62464\n\t"
        "v_mul_f32 v55, v25, v55\n\t"
        "v_lshlrev_b32 v29, 16, v29\n\t"
        "v_mfma_f32_16x16x32_bf16 v[72:75], v[208:211], v[104:107], v[72:75]\n\t"
        "v_sub_f32 v30, 1.0, v18\n\t"
        "v_mul_f32 v26, %[sgn], v18\n\t"
        "v_mfma_f32_16x16x32_bf16 v[88:91], v[212:215], v[104:107], v[88:91]\n\t"
        "v_cvt_pk_bf16_f32 v30, v30, v30\n\t"
        "v_lshlrev_b32 v21, 16, v248\n\t"
        "v_mfma_f32_16x16x32_bf16 v[76:79], v[208:211], v[108:111], v[76:79]\n\t"
        "v_and_b32 v22, 0xffff0000, v248\n\t"
        "v_sub_f32 v40, v40, v21\n\t"
        "v_mfma_f32_16x16x32_bf16 v[92:95], v[212:215], v[108:111], v[92:95]\n\t"
        "v_sub_f32 v41, v41, v22\n\t"
        "v_mul_f32 v40, v26, v40\n\t"
        "s_waitcnt vmcnt(13) lgkmcnt(0)\n\t"
        "v_mfma_f32_16x16x32_bf16 v[64:67], v[216:219], v[112:115], v[64:67]\n\t"
        "ds_read_b128 v[96:99], %[alds] offset:51200\n\t"
        "v_mul_f32 v41, v26, v41\n\t"
        "v_lshlrev_b32 v21, 16, v249\n\t"
        "v_mfma_f32_16x16x32_bf16 v[80:83], v[220:223], v[112:115], v[80:83]\n\t"
        "ds_read_b128 v[100:103], %[alds] offset:55296\n\t"
        "v_and_b32 v22, 0xffff0000, v249\n\t"
        "v_sub_f32 v42, v42, v21\n\t"
        "v_mfma_f32_16x16x32_bf16 v[68:71], v[216:219], v[116:119], v[68:71]\n\t"
        "ds_read_b128 v[104:107], %[alds] offset:59392\n\t"
        "v_sub_f32 v43, v43, v22\n\t"
        "v_mul_f32 v42, v26, v42\n\t"
        "v_mfma_f32_16x16x32_bf16 v[84:87], v[220:223], v[116:119], v[84:87]\n\t"
        "ds_read_b128 v[108:111], %[alds] offset:63488\n\t"
        "v_mul_f32 v43, v26, v43\n\t"
        "v_lshlrev_b32 v21, 16, v250\n\t"
        "v_mfma_f32_16x16x32_bf16 v[72:75], v[216:219], v[120:123], v[72:75]\n\t"
        "v_and_b32 v22, 0xffff0000, v250\n\t"
        "v_sub_f32 v56, v56, v21\n\t"
        "v_mfma_f32_16x16x32_bf16 v[88:91], v[220:223], v[120:123], v[88:91]\n\t"
        "v_sub_f32 v57, v57, v22\n\t"
        "v_mul_f32 v56, v26, v56\n\t"
        "v_mfma_f32_16x16x32_bf16 v[76:79], v[216:219], v[124:127], v[76:79]\n\t"
        "v_mul_f32 v57, v26, v57\n\t"
        "v_lshlrev_b32 v21, 16, v251\n\t"
        "v_mfma_f32_16x16x32_bf16 v[92:95], v[220:223], v[124:127], v[92:95]\n\t"
        "v_and_b32 v22, 0xffff0000, v251\n\t"
        "v_sub_f32 v58, v58, v21\n\t"
        "s_waitcnt vmcnt(11) lgkmcnt(0)\n\t"
        "v_mfma_f32_16x16x32_bf16 v[64:67], v[224:227], v[96:99], v[64:67]\n\t"
        "ds_read_b128 v[112:115], %[alds] offset:52224\n\t"
        "v_sub_f32 v59, v59, v22\n\t"
        "v_mul_f32 v58, v26, v58\n\t"
        "v_mfma_f32_16x16x32_bf16 v[80:83], v[228:231], v[96:99], v[80:83]\n\t"
        "ds_read_b128 v[116:119], %[alds] offset:56320\n\t"
        "v_mul_f32 v59, v26, v59\n\t"
        "v_lshlrev_b32 v30, 16, v30\n\t"
        "v_mfma_f32_16x16x32_bf16 v[68:71], v[224:227], v[100:103], v[68:71]\n\t"
        "ds_read_b128 v[120:123], %[alds] offset:60416\n\t"
        "v_sub_f32 v31, 1.0, v19\n\t"
        "v_mul_f32 v27, %[sgn], v19\n\t"
        "v_mfma_f32_16x16x32_bf16 v[84:87], v[228:231], v[100:103], v[84:87]\n\t"
        "ds_read_b128 v[124:127], %[alds] offset:64512\n\t"
        "v_cvt_pk_bf16_f32 v31, v31, v31\n\t"
        "v_lshlrev_b32 v21, 16, v252\n\t"
        "v_mfma_f32_16x16x32_bf16 v[72:75], v[224:227], v[104:107], v[72:75]\n\t"
        "v_and_b32 v22, 0xffff0000, v252\n\t"
        "v_sub_f32 v44, v44, v21\n\t"
        "v_mfma_f32_16x16x32_bf16 v[88:91], v[228:231], v[104:107], v[88:91]\n\t"
        "v_sub_f32 v45, v45, v22\n\t"
        "v_mul_f32 v44, v27, v44\n\t"
        "v_mfma_f32_16x16x32_bf16 v[76:79], v[224:227], v[108:111], v[76:79]\n\t"
        "v_mul_f32 v45, v27, v45\n\t"
        "v_lshlrev_b32 v21, 16, v253\n\t"
        "v_mfma_f32_16x16x32_bf16 v[92:95], v[228:231], v[108:111], v[92:95]\n\t"
        "v_and_b32 v22, 0xffff0000, v253\n\t"
        "v_sub_f32 v46, v46, v21\n\t"
        "s_waitcnt vmcnt(9) lgkmcnt(0)\n\t"
        "v_mfma_f32_16x16x32_bf16 v[64:67], v[232:235], v[112:115], v[64:67]\n\t"
        "v_sub_f32 v47, v47, v22\n\t"
        "v_mul_f32 v46, v27, v46\n\t"
        "v_mfma_f32_16x16x32_bf16 v[80:83], v[236:239], v[112:115], v[80:83]\n\t"
        "v_mul_f32 v47, v27, v47\n\t"
        "v_lshlrev_b32 v21, 16, v254\n\t"
        "v_mfma_f32_16x16x32_bf16 v[68:71], v[232:235], v[116:119], v[68:71]\n\t"
        "v_and_b32 v22, 0xffff0000, v254\n\t"
        "v_sub_f32 v60, v60, v21\n\t"
        "v_mfma_f32_16x16x32_bf16 v[84:87], v[236:239], v[116:119], v[84:87]\n\t"
        "v_sub_f32 v61, v61, v22\n\t"
        "v_mul_f32 v60, v27, v60\n\t"
        "v_mfma_f32_16x16x32_bf16 v[72:75], v[232:235], v[120:123], v[72:75]\n\t"
        "v_mul_f32 v61, v27, v61\n\t"
        "v_lshlrev_b32 v21, 16, v255\n\t"
        "v_mfma_f32_16x16x32_bf16 v[88:91], v[236:239], v[120:123], v[88:91]\n\t"
        "v_and_b32 v22, 0xffff0000, v255\n\t"
        "v_sub_f32 v62, v62, v21\n\t"
        "v_mfma_f32_16x16x32_bf16 v[76:79], v[232:235], v[124:127], v[76:79]\n\t"
        "v_sub_f32 v63, v63, v22\n\t"
        "v_mul_f32 v62, v27, v62\n\t"
        "v_mfma_f32_16x16x32_bf16 v[92:95], v[236:239], v[124:127], v[92:95]\n\t"
        "v_mul_f32 v63, v27, v63\n\t"
        "v_lshlrev_b32 v31, 16, v31\n\t"
        "s_waitcnt vmcnt(0) lgkmcnt(0)\n\t"
        "s_nop 7\n\t"
        : "=&{v[64:79]}"(ao0), "=&{v[80:95]}"(ao1), "=&{v[16:31]}"(pn), "+{v[32:47]}"(ay0), "+{v[48:63]}"(ay1), [fx]"=&v"(fx), [vfl]"+v"(vfl), "+{v[160:175]}"(wf0), "+{v[176:191]}"(wf1), "+{v[192:207]}"(wf2), "+{v[208:223]}"(wf3), "+{v[224:239]}"(wf4), "+{v[240:255]}"(wf5)
        : [vf]"v"(vf), [vo]"v"(vo), [alds]"v"(alds), [vz]"v"(vz), [cM]"s"(cM), [cL]"s"(cL), [po]"s"(po), [pf]"s"(pfo), [pfl]"s"(pfl), [tag]"s"(tag), [pg]"s"(pg), [px]"s"(pxi), [pfx]"s"(pfx), [pfy]"s"(pfy), [sgn]"v"(sgn), [sob]"s"(sob), [pre]"s"(pre), "v"(fw)
        : "v96", "v97", "v98", "v99", "v100", "v101", "v102", "v103", "v104", "v105", "v106", "v107", "v108", "v109", "v110", "v111", "v112", "v113", "v114", "v115", "v116", "v117", "v118", "v119", "v120", "v121", "v122", "v123", "v124", "v125", "v126", "v127", "v128", "v129", "v130", "v131", "v132", "v133", "v134", "v135", "v136", "v137", "v138", "v139", "v140", "v141", "v142", "v143", "v144", "v145", "v146", "v147", "v148", "v149", "v150", "v151", "v152", "v153", "v154", "v155", "v156", "v157", "v158", "v159", "s40", "s41", "s42", "s43", "s44", "s45", "s46", "s47", "s48", "s49", "s50", "s51", "s52", "s53", "s54", "s55", "vcc", "scc", "memory");
        const auto ro = __builtin_amdgcn_make_buffer_rsrc(out, 0, 2048 * 2048, 0x00020000);
#pragma unroll
        for (int tt = 0; tt < 4; ++tt) {
            const float gn = __uint_as_float(pn[12 + tt]);
            f32x16 t0, t1;
#pragma unroll
            for (int i = 0; i < 4; ++i) { t0[i] = __builtin_fmaf(gn, ao0[4 * tt + i], ay0[4 * tt + i]); t1[i] = __builtin_fmaf(gn, ao1[4 * tt + i], ay1[4 * tt + i]); }
            __builtin_amdgcn_raw_buffer_store_b128(pack44(t0, 0, t1, 0), ro, pn[4] + tt * 16 * 2048, 0, WT); }
    } else {
        LANE_VALUES SRC_A(lane)
        const u16* pw = wqkv;
        __builtin_amdgcn_s_setprio(PRIO_A1);
        {
            const int row = 64 * m + 16 * (i16 >> 3) + (i16 & 7);
            const unsigned ax0 = lds0 + row * 128 + ((g4 ^ fsw(row)) * 16), ax1 = lds0 + row * 128 + (((4 + g4) ^ fsw(row)) * 16);
            const int q = i16 >> 2, p = lane & 3, fk = ((q >> 1) << 1) | (g4 & 1);
            const unsigned aw0 = lds0 + 32768 + (8 * g4 + q) * 128 + (((2 * c) ^ fk) * 32) + p * 8, aw1 = lds0 + 32768 + (8 * g4 + q) * 128 + (((2 * c + 1) ^ fk) * 32) + p * 8;
            f32x16 acc0, acc1, acc2, acc3, acc4, acc5;
    asm volatile(
        "global_load_dword v136, %[pfa], off\n\t"
        "global_load_dwordx2 %[tagv], %[vep], %[ws] sc1\n\t"
        "s_mov_b64 s[40:41], %[px]\n\t"
        "s_and_b32 s41, s41, 0xffff\n\t"
        "s_mov_b32 s42, 0x400000\n\t"
        "s_mov_b32 s43, 0x20000\n\t"
        "s_mov_b64 s[44:45], %[pw]\n\t"
        "s_and_b32 s45, s45, 0xffff\n\t"
        "s_mov_b32 s46, 0x600000\n\t"
        "s_mov_b32 s47, 0x20000\n\t"
        "s_mov_b32 s48, 256\n\t"
        "s_mov_b32 s49, 786432\n\t"
        "s_mov_b32 s50, 788480\n\t"
        "s_mov_b32 s51, 790528\n\t"
        "s_mov_b32 s52, 0\n\t"
        "v_mov_b32 v160, 0\n\t"
        "v_mov_b32 v161, 0\n\t"
        "v_mov_b32 v162, 0\n\t"
        "v_mov_b32 v163, 0\n\t"
        "v_mov_b32 v164, 0\n\t"
        "v_mov_b32 v165, 0\n\t"
        "v_mov_b32 v166, 0\n\t"
        "v_mov_b32 v167, 0\n\t"
        "v_mov_b32 v168, 0\n\t"
        "v_mov_b32 v169, 0\n\t"
        "v_mov_b32 v170, 0\n\t"
        "v_mov_b32 v171, 0\n\t"
        "v_mov_b32 v172, 0\n\t"
        "v_mov_b32 v173, 0\n\t"
        "v_mov_b32 v174, 0\n\t"
        "v_mov_b32 v175, 0\n\t"
        "v_mov_b32 v176, 0\n\t"
        "v_mov_b32 v177, 0\n\t"
        "v_mov_b32 v178, 0\n\t"
        "v_mov_b32 v179, 0\n\t"
        "v_mov_b32 v180, 0\n\t"
        "v_mov_b32 v181, 0\n\t"
        "v_mov_b32 v182, 0\n\t"
        "v_mov_b32 v183, 0\n\t"
        "v_mov_b32 v184, 0\n\t"
        "v_mov_b32 v185, 0\n\t"
        "v_mov_b32 v186, 0\n\t"
        "v_mov_b32 v187, 0\n\t"
        "v_mov_b32 v188, 0\n\t"
        "v_mov_b32 v189, 0\n\t"
        "v_mov_b32 v190, 0\n\t"
        "v_mov_b32 v191, 0\n\t"
        "v_mov_b32 v192, 0\n\t"
        "v_mov_b32 v193, 0\n\t"
        "v_mov_b32 v194, 0\n\t"
        "v_mov_b32 v195, 0\n\t"
        "v_mov_b32 v196, 0\n\t"
        "v_mov_b32 v197, 0\n\t"
        "v_mov_b32 v198, 0\n\t"
        "v_mov_b32 v199, 0\n\t"
        "v_mov_b32 v200, 0\n\t"
        "v_mov_b32 v201, 0\n\t"
        "v_mov_b32 v202, 0\n\t"
        "v_mov_b32 v203, 0\n\t"
        "v_mov_b32 v204, 0\n\t"
        "v_mov_b32 v205, 0\n\t"
        "v_mov_b32 v206, 0\n\t"
        "v_mov_b32 v207, 0\n\t"
        "v_mov_b32 v208, 0\n\t"
        "v_mov_b32 v209, 0\n\t"
        "v_mov_b32 v210, 0\n\t"
        "v_mov_b32 v211, 0\n\t"
        "v_mov_b32 v212, 0\n\t"
        "v_mov_b32 v213, 0\n\t"
        "v_mov_b32 v214, 0\n\t"
        "v_mov_b32 v215, 0\n\t"
        "v_mov_b32 v216, 0\n\t"
        "v_mov_b32 v217, 0\n\t"
        "v_mov_b32 v218, 0\n\t"
        "v_mov_b32 v219, 0\n\t"
        "v_mov_b32 v220, 0\n\t"
        "v_mov_b32 v221, 0\n\t"
        "v_mov_b32 v222, 0\n\t"
        "v_mov_b32 v223, 0\n\t"
        "v_mov_b32 v224, 0\n\t"
        "v_mov_b32 v225, 0\n\t"
        "v_mov_b32 v226, 0\n\t"
        "v_mov_b32 v227, 0\n\t"
        "v_mov_b32 v228, 0\n\t"
        "v_mov_b32 v229, 0\n\t"
        "v_mov_b32 v230, 0\n\t"
        "v_mov_b32 v231, 0\n\t"
        "v_mov_b32 v232, 0\n\t"
        "v_mov_b32 v233, 0\n\t"
        "v_mov_b32 v234, 0\n\t"
        "v_mov_b32 v235, 0\n\t"
        "v_mov_b32 v236, 0\n\t"
        "v_mov_b32 v237, 0\n\t"
        "v_mov_b32 v238, 0\n\t"
        "v_mov_b32 v239, 0\n\t"
        "v_mov_b32 v240, 0\n\t"
        "v_mov_b32 v241, 0\n\t"
        "v_mov_b32 v242, 0\n\t"
        "v_mov_b32 v243, 0\n\t"
        "v_mov_b32 v244, 0\n\t"
        "v_mov_b32 v245, 0\n\t"
        "v_mov_b32 v246, 0\n\t"
        "v_mov_b32 v247, 0\n\t"
        "v_mov_b32 v248, 0\n\t"
        "v_mov_b32 v249, 0\n\t"
        "v_mov_b32 v250, 0\n\t"
        "v_mov_b32 v251, 0\n\t"
        "v_mov_b32 v252, 0\n\t"
        "v_mov_b32 v253, 0\n\t"
        "v_mov_b32 v254, 0\n\t"
        "v_mov_b32 v255, 0\n\t"
        "s_waitcnt vmcnt(15)\n\t"
        "s_barrier\n\t"
        "ds_read_b128 v[40:43], %[ax0] offset:0\n\t"
        "ds_read_b64_tr_b16 v[88:89], %[aw0] offset:0\n\t"
        "ds_read_b64_tr_b16 v[90:91], %[aw0] offset:512\n\t"
        "ds_read_b128 v[44:47], %[ax0] offset:1024\n\t"
        "ds_read_b128 v[48:51], %[ax0] offset:4096\n\t"
        "ds_read_b128 v[52:55], %[ax0] offset:5120\n\t"
        "ds_read_b64_tr_b16 v[92:93], %[aw1] offset:0\n\t"
        "ds_read_b64_tr_b16 v[94:95], %[aw1] offset:512\n\t"
        "ds_read_b64_tr_b16 v[96:97], %[aw0] offset:4096\n\t"
        "ds_read_b64_tr_b16 v[98:99], %[aw0] offset:4608\n\t"
        "ds_read_b64_tr_b16 v[100:101], %[aw1] offset:4096\n\t"
        "ds_read_b64_tr_b16 v[102:103], %[aw1] offset:4608\n\t"
        "ds_read_b64_tr_b16 v[104:105], %[aw0] offset:8192\n\t"
        "ds_read_b64_tr_b16 v[106:107], %[aw0] offset:8704\n\t"
        "ds_read_b64_tr_b16 v[108:109], %[aw1] offset:8192\n\t"
        "ds_read_b64_tr_b16 v[110:111], %[aw1] offset:8704\n\t"
        "ds_read_b128 v[56:59], %[ax1] offset:0\n\t"
        "ds_read_b128 v[60:63], %[ax1] offset:1024\n\t"
        "ds_read_b128 v[64:67], %[ax1] offset:4096\n\t"
        "ds_read_b128 v[68:71], %[ax1] offset:5120\n\t"
        "1:\n\t"
        "s_cmp_ge_u32 s52, 7\n\t"
        "s_cselect_b32 s42, 0, s42\n\t"
        "s_cselect_b32 s46, 0, s46\n\t"
        "s_waitcnt vmcnt(10) lgkmcnt(0)\n\t"
        "s_barrier\n\t"
        "v_mfma_f32_16x16x32_bf16 v[160:163], v[88:91], v[40:43], v[160:163]\n\t"
        "s_add_u32 m0, %[cX], 0\n\t"
        "ds_read_b64_tr_b16 v[112:113], %[aw0] offset:12288\n\t"
        "v_mfma_f32_16x16x32_bf16 v[164:167], v[88:91], v[44:47], v[164:167]\n\t"
        "buffer_load_dwordx4 %[vx0], s[40:43], s48 offen offset:0 lds\n\t"
        "ds_read_b64_tr_b16 v[114:115], %[aw0] offset:12800\n\t"
        "v_mfma_f32_16x16x32_bf16 v[168:171], v[88:91], v[48:51], v[168:171]\n\t"
        "v_mfma_f32_16x16x32_bf16 v[172:175], v[88:91], v[52:55], v[172:175]\n\t"
        "ds_read_b64_tr_b16 v[116:117], %[aw1] offset:12288\n\t"
        "v_mfma_f32_16x16x32_bf16 v[188:191], v[92:95], v[52:55], v[188:191]\n\t"
        "buffer_load_dwordx4 %[vx1], s[40:43], s48 offen offset:1024 lds\n\t"
        "v_mfma_f32_16x16x32_bf16 v[184:187], v[92:95], v[48:51], v[184:187]\n\t"
        "ds_read_b64_tr_b16 v[118:119], %[aw1] offset:12800\n\t"
        "v_mfma_f32_16x16x32_bf16 v[180:183], v[92:95], v[44:47], v[180:183]\n\t"
        "v_mfma_f32_16x16x32_bf16 v[176:179], v[92:95], v[40:43], v[176:179]\n\t"
        "buffer_load_dwordx4 %[vx2], s[40:43], s48 offen offset:2048 lds\n\t"
        "ds_read_b64_tr_b16 v[120:121], %[aw0] offset:16384\n\t"
        "v_mfma_f32_16x16x32_bf16 v[192:195], v[96:99], v[40:43], v[192:195]\n\t"
        "ds_read_b64_tr_b16 v[122:123], %[aw0] offset:16896\n\t"
        "v_mfma_f32_16x16x32_bf16 v[196:199], v[96:99], v[44:47], v[196:199]\n\t"
        "v_mfma_f32_16x16x32_bf16 v[200:203], v[96:99], v[48:51], v[200:203]\n\t"
        "buffer_load_dwordx4 %[vx3], s[40:43], s48 offen offset:3072 lds\n\t"
        "ds_read_b64_tr_b16 v[124:125], %[aw1] offset:16384\n\t"
        "v_mfma_f32_16x16x32_bf16 v[204:207], v[96:99], v[52:55], v[204:207]\n\t"
        "v_mfma_f32_16x16x32_bf16 v[220:223], v[100:103], v[52:55], v[220:223]\n\t"
        "ds_read_b64_tr_b16 v[126:127], %[aw1] offset:16896\n\t"
        "v_mfma_f32_16x16x32_bf16 v[216:219], v[100:103], v[48:51], v[216:219]\n\t"
        "s_add_u32 m0, %[cW], 0\n\t"
        "v_mfma_f32_16x16x32_bf16 v[212:215], v[100:103], v[44:47], v[212:215]\n\t"
        "buffer_load_dwordx4 %[vw0], s[44:47], s49 offen offset:0 lds\n\t"
        "ds_read_b64_tr_b16 v[128:129], %[aw0] offset:20480\n\t"
        "v_mfma_f32_16x16x32_bf16 v[208:211], v[100:103], v[40:43], v[208:211]\n\t"
        "ds_read_b64_tr_b16 v[130:131], %[aw0] offset:20992\n\t"
        "v_mfma_f32_16x16x32_bf16 v[224:227], v[40:43], v[104:107], v[224:227]\n\t"
        "s_add_u32 m0, %[cW], 4096\n\t"
        "v_mfma_f32_16x16x32_bf16 v[228:231], v[44:47], v[104:107], v[228:231]\n\t"
        "buffer_load_dwordx4 %[vw0], s[44:47], s50 offen offset:0 lds\n\t"
        "ds_read_b64_tr_b16 v[132:133], %[aw1] offset:20480\n\t"
        "v_mfma_f32_16x16x32_bf16 v[232:235], v[48:51], v[104:107], v[232:235]\n\t"
        "v_mfma_f32_16x16x32_bf16 v[236:239], v[52:55], v[104:107], v[236:239]\n\t"
        "s_add_u32 m0, %[cW], 8192\n\t"
        "ds_read_b64_tr_b16 v[134:135], %[aw1] offset:20992\n\t"
        "v_mfma_f32_16x16x32_bf16 v[252:255], v[52:55], v[108:111], v[252:255]\n\t"
        "buffer_load_dwordx4 %[vw0], s[44:47], s51 offen offset:0 lds\n\t"
        "s_add_u32 s48, s48, 128\n\t"
        "s_add_u32 s49, s49, 196608\n\t"
        "s_add_u32 s50, s50, 196608\n\t"
        "s_add_u32 s51, s51, 196608\n\t"
        "v_mfma_f32_16x16x32_bf16 v[248:251], v[48:51], v[108:111], v[248:251]\n\t"
        "v_mfma_f32_16x16x32_bf16 v[244:247], v[44:47], v[108:111], v[244:247]\n\t"
        "v_mfma_f32_16x16x32_bf16 v[240:243], v[40:43], v[108:111], v[240:243]\n\t"
        "s_waitcnt vmcnt(10) lgkmcnt(0)\n\t"
        "s_barrier\n\t"
        "v_mfma_f32_16x16x32_bf16 v[160:163], v[112:115], v[56:59], v[160:163]\n\t"
        "s_add_u32 m0, %[cW], 12288\n\t"
        "ds_read_b128 v[40:43], %[ax0] offset:16384\n\t"
        "v_mfma_f32_16x16x32_bf16 v[164:167], v[112:115], v[60:63], v[164:167]\n\t"
        "buffer_load_dwordx4 %[vw0], s[44:47], s49 offen offset:0 lds\n\t"
        "ds_read_b64_tr_b16 v[88:89], %[aw0] offset:24576\n\t"
        "v_mfma_f32_16x16x32_bf16 v[168:171], v[112:115], v[64:67], v[168:171]\n\t"
        "ds_read_b64_tr_b16 v[90:91], %[aw0] offset:25088\n\t"
        "v_mfma_f32_16x16x32_bf16 v[172:175], v[112:115], v[68:71], v[172:175]\n\t"
        "ds_read_b128 v[44:47], %[ax0] offset:17408\n\t"
        "v_mfma_f32_16x16x32_bf16 v[188:191], v[116:119], v[68:71], v[188:191]\n\t"
        "ds_read_b128 v[48:51], %[ax0] offset:20480\n\t"
        "v_mfma_f32_16x16x32_bf16 v[184:187], v[116:119], v[64:67], v[184:187]\n\t"
        "ds_read_b128 v[52:55], %[ax0] offset:21504\n\t"
        "v_mfma_f32_16x16x32_bf16 v[180:183], v[116:119], v[60:63], v[180:183]\n\t"
        "ds_read_b64_tr_b16 v[92:93], %[aw1] offset:24576\n\t"
        "v_mfma_f32_16x16x32_bf16 v[176:179], v[116:119], v[56:59], v[176:179]\n\t"
        "s_add_u32 m0, %[cW], 16384\n\t"
        "ds_read_b64_tr_b16 v[94:95], %[aw1] offset:25088\n\t"
        "v_mfma_f32_16x16x32_bf16 v[192:195], v[120:123], v[56:59], v[192:195]\n\t"
        "buffer_load_dwordx4 %[vw0], s[44:47], s50 offen offset:0 lds\n\t"
        "ds_read_b64_tr_b16 v[96:97], %[aw0] offset:28672\n\t"
        "v_mfma_f32_16x16x32_bf16 v[196:199], v[120:123], v[60:63], v[196:199]\n\t"
        "ds_read_b64_tr_b16 v[98:99], %[aw0] offset:29184\n\t"
        "v_mfma_f32_16x16x32_bf16 v[200:203], v[120:123], v[64:67], v[200:203]\n\t"
        "ds_read_b64_tr_b16 v[100:101], %[aw1] offset:28672\n\t"
        "v_mfma_f32_16x16x32_bf16 v[204:207], v[120:123], v[68:71], v[204:207]\n\t"
        "ds_read_b64_tr_b16 v[102:103], %[aw1] offset:29184\n\t"
        "v_mfma_f32_16x16x32_bf16 v[220:223], v[124:127], v[68:71], v[220:223]\n\t"
        "ds_read_b64_tr_b16 v[104:105], %[aw0] offset:32768\n\t"
        "v_mfma_f32_16x16x32_bf16 v[216:219], v[124:127], v[64:67], v[216:219]\n\t"
        "ds_read_b64_tr_b16 v[106:107], %[aw0] offset:33280\n\t"
        "v_mfma_f32_16x16x32_bf16 v[212:215], v[124:127], v[60:63], v[212:215]\n\t"
        "ds_read_b64_tr_b16 v[108:109], %[aw1] offset:32768\n\t"
        "v_mfma_f32_16x16x32_bf16 v[208:211], v[124:127], v[56:59], v[208:211]\n\t"
        "s_add_u32 m0, %[cW], 20480\n\t"
        "ds_read_b64_tr_b16 v[110:111], %[aw1] offset:33280\n\t"
        "v_mfma_f32_16x16x32_bf16 v[224:227], v[56:59], v[128:131], v[224:227]\n\t"
        "buffer_load_dwordx4 %[vw0], s[44:47], s51 offen offset:0 lds\n\t"
        "s_add_u32 s49, s49, 196608\n\t"
        "s_add_u32 s50, s50, 196608\n\t"
        "s_add_u32 s51, s51, 196608\n\t"
        "ds_read_b128 v[72:75], %[ax1] offset:16384\n\t"
        "v_mfma_f32_16x16x32_bf16 v[228:231], v[60:63], v[128:131], v[228:231]\n\t"
        "ds_read_b128 v[76:79], %[ax1] offset:17408\n\t"
        "v_mfma_f32_16x16x32_bf16 v[232:235], v[64:67], v[128:131], v[232:235]\n\t"
        "ds_read_b128 v[80:83], %[ax1] offset:20480\n\t"
        "v_mfma_f32_16x16x32_bf16 v[236:239], v[68:71], v[128:131], v[236:239]\n\t"
        "ds_read_b128 v[84:87], %[ax1] offset:21504\n\t"
        "v_mfma_f32_16x16x32_bf16 v[252:255], v[68:71], v[132:135], v[252:255]\n\t"
        "v_mfma_f32_16x16x32_bf16 v[248:251], v[64:67], v[132:135], v[248:251]\n\t"
        "v_mfma_f32_16x16x32_bf16 v[244:247], v[60:63], v[132:135], v[244:247]\n\t"
        "v_mfma_f32_16x16x32_bf16 v[240:243], v[56:59], v[132:135], v[240:243]\n\t"
        "s_waitcnt vmcnt(10) lgkmcnt(0)\n\t"
        "s_barrier\n\t"
        "v_mfma_f32_16x16x32_bf16 v[160:163], v[88:91], v[40:43], v[160:163]\n\t"
        "s_add_u32 m0, %[cX], 16384\n\t"
        "ds_read_b64_tr_b16 v[112:113], %[aw0] offset:36864\n\t"
        "v_mfma_f32_16x16x32_bf16 v[164:167], v[88:91], v[44:47], v[164:167]\n\t"
        "buffer_load_dwordx4 %[vx0], s[40:43], s48 offen offset:0 lds\n\t"
        "ds_read_b64_tr_b16 v[114:115], %[aw0] offset:37376\n\t"
        "v_mfma_f32_16x16x32_bf16 v[168:171], v[88:91], v[48:51], v[168:171]\n\t"
        "v_mfma_f32_16x16x32_bf16 v[172:175], v[88:91], v[52:55], v[172:175]\n\t"
        "ds_read_b64_tr_b16 v[116:117], %[aw1] offset:36864\n\t"
        "v_mfma_f32_16x16x32_bf16 v[188:191], v[92:95], v[52:55], v[188:191]\n\t"
        "buffer_load_dwordx4 %[vx1], s[40:43], s48 offen offset:1024 lds\n\t"
        "v_mfma_f32_16x16x32_bf16 v[184:187], v[92:95], v[48:51], v[184:187]\n\t"
        "ds_read_b64_tr_b16 v[118:119], %[aw1] offset:37376\n\t"
        "v_mfma_f32_16x16x32_bf16 v[180:183], v[92:95], v[44:47], v[180:183]\n\t"
        "v_mfma_f32_16x16x32_bf16 v[176:179], v[92:95], v[40:43], v[176:179]\n\t"
        "buffer_load_dwordx4 %[vx2], s[40:43], s48 offen offset:2048 lds\n\t"
        "ds_read_b64_tr_b16 v[120:121], %[aw0] offset:40960\n\t"
        "v_mfma_f32_16x16x32_bf16 v[192:195], v[96:99], v[40:43], v[192:195]\n\t"
        "ds_read_b64_tr_b16 v[122:123], %[aw0] offset:41472\n\t"
        "v_mfma_f32_16x16x32_bf16 v[196:199], v[96:99], v[44:47], v[196:199]\n\t"
        "v_mfma_f32_16x16x32_bf16 v[200:203], v[96:99], v[48:51], v[200:203]\n\t"
        "buffer_load_dwordx4 %[vx3], s[40:43], s48 offen offset:3072 lds\n\t"
        "ds_read_b64_tr_b16 v[124:125], %[aw1] offset:40960\n\t"
        "v_mfma_f32_16x16x32_bf16 v[204:207], v[96:99], v[52:55], v[204:207]\n\t"
        "v_mfma_f32_16x16x32_bf16 v[220:223], v[100:103], v[52:55], v[220:223]\n\t"
        "ds_read_b64_tr_b16 v[126:127], %[aw1] offset:41472\n\t"
        "v_mfma_f32_16x16x32_bf16 v[216:219], v[100:103], v[48:51], v[216:219]\n\t"
        "s_add_u32 m0, %[cW], 24576\n\t"
        "v_mfma_f32_16x16x32_bf16 v[212:215], v[100:103], v[44:47], v[212:215]\n\t"
        "buffer_load_dwordx4 %[vw0], s[44:47], s49 offen offset:0 lds\n\t"
        "ds_read_b64_tr_b16 v[128:129], %[aw0] offset:45056\n\t"
        "v_mfma_f32_16x16x32_bf16 v[208:211], v[100:103], v[40:43], v[208:211]\n\t"
        "ds_read_b64_tr_b16 v[130:131], %[aw0] offset:45568\n\t"
        "v_mfma_f32_16x16x32_bf16 v[224:227], v[40:43], v[104:107], v[224:227]\n\t"
        "s_add_u32 m0, %[cW], 28672\n\t"
        "v_mfma_f32_16x16x32_bf16 v[228:231], v[44:47], v[104:107], v[228:231]\n\t"
        "buffer_load_dwordx4 %[vw0], s[44:47], s50 offen offset:0 lds\n\t"
        "ds_read_b64_tr_b16 v[132:133], %[aw1] offset:45056\n\t"
        "v_mfma_f32_16x16x32_bf16 v[232:235], v[48:51], v[104:107], v[232:235]\n\t"
        "v_mfma_f32_16x16x32_bf16 v[236:239], v[52:55], v[104:107], v[236:239]\n\t"
        "s_add_u32 m0, %[cW], 32768\n\t"
        "ds_read_b64_tr_b16 v[134:135], %[aw1] offset:45568\n\t"
        "v_mfma_f32_16x16x32_bf16 v[252:255], v[52:55], v[108:111], v[252:255]\n\t"
        "buffer_load_dwordx4 %[vw0], s[44:47], s51 offen offset:0 lds\n\t"
        "s_add_u32 s48, s48, 128\n\t"
        "s_add_u32 s49, s49, 196608\n\t"
        "s_add_u32 s50, s50, 196608\n\t"
        "s_add_u32 s51, s51, 196608\n\t"
        "v_mfma_f32_16x16x32_bf16 v[248:251], v[48:51], v[108:111], v[248:251]\n\t"
        "v_mfma_f32_16x16x32_bf16 v[244:247], v[44:47], v[108:111], v[244:247]\n\t"
        "v_mfma_f32_16x16x32_bf16 v[240:243], v[40:43], v[108:111], v[240:243]\n\t"
        "s_waitcnt vmcnt(10) lgkmcnt(0)\n\t"
        "s_barrier\n\t"
        "v_mfma_f32_16x16x32_bf16 v[160:163], v[112:115], v[72:75], v[160:163]\n\t"
        "s_add_u32 m0, %[cW], 36864\n\t"
        "ds_read_b128 v[40:43], %[ax0] offset:0\n\t"
        "v_mfma_f32_16x16x32_bf16 v[164:167], v[112:115], v[76:79], v[164:167]\n\t"
        "buffer_load_dwordx4 %[vw0], s[44:47], s49 offen offset:0 lds\n\t"
        "ds_read_b64_tr_b16 v[88:89], %[aw0] offset:0\n\t"
        "v_mfma_f32_16x16x32_bf16 v[168:171], v[112:115], v[80:83], v[168:171]\n\t"
        "ds_read_b64_tr_b16 v[90:91], %[aw0] offset:512\n\t"
        "v_mfma_f32_16x16x32_bf16 v[172:175], v[112:115], v[84:87], v[172:175]\n\t"
        "ds_read_b128 v[44:47], %[ax0] offset:1024\n\t"
        "v_mfma_f32_16x16x32_bf16 v[188:191], v[116:119], v[84:87], v[188:191]\n\t"
        "ds_read_b128 v[48:51], %[ax0] offset:4096\n\t"
        "v_mfma_f32_16x16x32_bf16 v[184:187], v[116:119], v[80:83], v[184:187]\n\t"
        "ds_read_b128 v[52:55], %[ax0] offset:5120\n\t"
        "v_mfma_f32_16x16x32_bf16 v[180:183], v[116:119], v[76:79], v[180:183]\n\t"
        "ds_read_b64_tr_b16 v[92:93], %[aw1] offset:0\n\t"
        "v_mfma_f32_16x16x32_bf16 v[176:179], v[116:119], v[72:75], v[176:179]\n\t"
        "s_add_u32 m0, %[cW], 40960\n\t"
        "ds_read_b64_tr_b16 v[94:95], %[aw1] offset:512\n\t"
        "v_mfma_f32_16x16x32_bf16 v[192:195], v[120:123], v[72:75], v[192:195]\n\t"
        "buffer_load_dwordx4 %[vw0], s[44:47], s50 offen offset:0 lds\n\t"
        "ds_read_b64_tr_b16 v[96:97], %[aw0] offset:4096\n\t"
        "v_mfma_f32_16x16x32_bf16 v[196:199], v[120:123], v[76:79], v[196:199]\n\t"
        "ds_read_b64_tr_b16 v[98:99], %[aw0] offset:4608\n\t"
        "v_mfma_f32_16x16x32_bf16 v[200:203], v[120:123], v[80:83], v[200:203]\n\t"
        "ds_read_b64_tr_b16 v[100:101], %[aw1] offset:4096\n\t"
        "v_mfma_f32_16x16x32_bf16 v[204:207], v[120:123], v[84:87], v[204:207]\n\t"
        "ds_read_b64_tr_b16 v[102:103], %[aw1] offset:4608\n\t"
        "v_mfma_f32_16x16x32_bf16 v[220:223], v[124:127], v[84:87], v[220:223]\n\t"
        "ds_read_b64_tr_b16 v[104:105], %[aw0] offset:8192\n\t"
        "v_mfma_f32_16x16x32_bf16 v[216:219], v[124:127], v[80:83], v[216:219]\n\t"
        "ds_read_b64_tr_b16 v[106:107], %[aw0] offset:8704\n\t"
        "v_mfma_f32_16x16x32_bf16 v[212:215], v[124:127], v[76:79], v[212:215]\n\t"
        "ds_read_b64_tr_b16 v[108:109], %[aw1] offset:8192\n\t"
        "v_mfma_f32_16x16x32_bf16 v[208:211], v[124:127], v[72:75], v[208:211]\n\t"
        "s_add_u32 m0, %[cW], 45056\n\t"
        "ds_read_b64_tr_b16 v[110:111], %[aw1] offset:8704\n\t"
        "v_mfma_f32_16x16x32_bf16 v[224:227], v[72:75], v[128:131], v[224:227]\n\t"
        "buffer_load_dwordx4 %[vw0], s[44:47], s51 offen offset:0 lds\n\t"
        "s_add_u32 s49, s49, 196608\n\t"
        "s_add_u32 s50, s50, 196608\n\t"
        "s_add_u32 s51, s51, 196608\n\t"
        "ds_read_b128 v[56:59], %[ax1] offset:0\n\t"
        "v_mfma_f32_16x16x32_bf16 v[228:231], v[76:79], v[128:131], v[228:231]\n\t"
        "ds_read_b128 v[60:63], %[ax1] offset:1024\n\t"
        "v_mfma_f32_16x16x32_bf16 v[232:235], v[80:83], v[128:131], v[232:235]\n\t"
        "ds_read_b128 v[64:67], %[ax1] offset:4096\n\t"
        "v_mfma_f32_16x16x32_bf16 v[236:239], v[84:87], v[128:131], v[236:239]\n\t"
        "ds_read_b128 v[68:71], %[ax1] offset:5120\n\t"
        "v_mfma_f32_16x16x32_bf16 v[252:255], v[84:87], v[132:135], v[252:255]\n\t"
        "v_mfma_f32_16x16x32_bf16 v[248:251], v[80:83], v[132:135], v[248:251]\n\t"
        "v_mfma_f32_16x16x32_bf16 v[244:247], v[76:79], v[132:135], v[244:247]\n\t"
        "v_mfma_f32_16x16x32_bf16 v[240:243], v[72:75], v[132:135], v[240:243]\n\t"
        "s_add_u32 s52, s52, 1\n\t"
        "s_cmp_lt_u32 s52, 8\n\t"
        "s_cbranch_scc1 1b\n\t"
        "s_waitcnt vmcnt(0) lgkmcnt(0)\n\t"
        "s_barrier\n\t"
        "s_nop 7\n\t"
        : "=&{v[160:175]}"(acc0), "=&{v[176:191]}"(acc1), "=&{v[192:207]}"(acc2), "=&{v[208:223]}"(acc3), "=&{v[224:239]}"(acc4), "=&{v[240:255]}"(acc5), [tagv]"=&v"(tagv)
        : [ax0]"v"(ax0), [ax1]"v"(ax1), [aw0]"v"(aw0), [aw1]"v"(aw1), [vx0]"v"(vx0), [vx1]"v"(vx1), [vx2]"v"(vx2), [vx3]"v"(vx3), [vw0]"v"(vw0), [cX]"s"(cX), [cW]"s"(cW), [px]"s"(px), [pw]"s"(pw), [pfa]"v"(pwarm), [vep]"v"(vep), [ws]"s"(ws)
        : "v40", "v41", "v42", "v43", "v44", "v45", "v46", "v47", "v48", "v49", "v50", "v51", "v52", "v53", "v54", "v55", "v56", "v57", "v58", "v59", "v60", "v61", "v62", "v63", "v64", "v65", "v66", "v67", "v68", "v69", "v70", "v71", "v72", "v73", "v74", "v75", "v76", "v77", "v78", "v79", "v80", "v81", "v82", "v83", "v84", "v85", "v86", "v87", "v88", "v89", "v90", "v91", "v92", "v93", "v94", "v95", "v96", "v97", "v98", "v99", "v100", "v101", "v102", "v103", "v104", "v105", "v106", "v107", "v108", "v109", "v110", "v111", "v112", "v113", "v114", "v115", "v116", "v117", "v118", "v119", "v120", "v121", "v122", "v123", "v124", "v125", "v126", "v127", "v128", "v129", "v130", "v131", "v132", "v133", "v134", "v135", "v136", "s40", "s41", "s42", "s43", "s44", "s45", "s46", "s47", "s48", "s49", "s50", "s51", "s52", "scc", "memory");
            tagv += 1;
            const int h = 2 * cg + c, blk = 2 * rblk1;
            const unsigned okv = WS_KV + (h * 64 + blk) * 4096 + lane * 16, oq = WS_QF + (h * 64 + blk) * 2048 + (i16 >> 3) * 1024 + ((i16 & 7) + 16 * g4) * 16;
#pragma unroll
            for (int st = 0; st < 4; ++st) {
                __builtin_amdgcn_raw_buffer_store_b128(pack44(acc2, 4 * st, acc3, 4 * st), rs, okv + (st >> 1) * 4096 + (st & 1) * 1024, 0, WT);
                __builtin_amdgcn_raw_buffer_store_b128(pack44(acc0, 4 * st, acc1, 4 * st, QSCALE), rs, oq + (st >> 1) * 2048 + (st & 1) * 128, 0, WT); }
#pragma unroll
            for (int gp = 0; gp < 2; ++gp) {
                __builtin_amdgcn_raw_buffer_store_b128(pack44(acc4, 8 * gp, acc4, 8 * gp + 4), rs, okv + gp * 4096 + 2048, 0, WT);
                __builtin_amdgcn_raw_buffer_store_b128(pack44(acc5, 8 * gp, acc5, 8 * gp + 4), rs, okv + gp * 4096 + 3072, 0, WT); }
            asm volatile("s_waitcnt vmcnt(0)" ::: "memory");
            if (lane == 0) __hip_atomic_store((GAS u64*)(ws + WS_FA) + h * 32 + rblk1, tagv, RLX);
        }
        const int r = lane & 31, hh = lane >> 5;
        const int h = 4 * xcd + (jj >> 3), rblk = 4 * (jj & 7) + w;
        __builtin_amdgcn_s_setprio(0);
        {
            const int grow = rblk * 64 + 2 * h + hh;
            const u16* pg = x + (size_t)grow * 1024 + r * 8;
            const u32x4 g0 = *(const u32x4*)pg, g1 = *(const u32x4*)(pg + 256), g2 = *(const u32x4*)(pg + 512), g3 = *(const u32x4*)(pg + 768);
            const u32x4 w0 = *(const u32x4*)(gw + r * 8), w1 = *(const u32x4*)(gw + 256 + r * 8), w2 = *(const u32x4*)(gw + 512 + r * 8), w3 = *(const u32x4*)(gw + 768 + r * 8);
            const u16* pwo = wo + (size_t)(h * 32 + 4 * g4) * 1024 + rblk * 32 + 8 * (i16 >> 2) + (i16 & 3);
            unsigned t[16];
#pragma unroll
            for (int i = 0; i < 16; ++i) t[i] = pwo[(16 * ((i >> 2) & 1) + (i & 3)) * 1024 + 4 * (i >> 3)];
#pragma unroll
            for (int u = 0; u < 2; ++u) {
                u32x4 o; o[0] = t[8 * u] | (t[8 * u + 1] << 16); o[1] = t[8 * u + 2] | (t[8 * u + 3] << 16); o[2] = t[8 * u + 4] | (t[8 * u + 5] << 16); o[3] = t[8 * u + 6] | (t[8 * u + 7] << 16);
                __builtin_amdgcn_raw_buffer_store_b128(o, rs, WS_FF + rblk * 327680 + (128 + h) * 2048 + u * 1024 + lane * 16, 0, WT); }
            float s = 0.f;
#pragma unroll
            for (int i = 0; i < 4; ++i) { s = dot2(g0[i], w0[i], s); s = dot2(g1[i], w1[i], s); s = dot2(g2[i], w2[i], s); s = dot2(g3[i], w3[i], s); }
#pragma unroll
            for (int o2 = 16; o2 > 0; o2 >>= 1) s += __shfl_xor(s, o2);
            const float gt = rbf(1.f / (1.f + __expf(-rbf(s))));
            if (r == 0) __hip_atomic_store((GAS unsigned*)(ws + WS_G) + rblk * 64 + (grow & 15) * 4 + ((grow >> 4) & 3), __float_as_uint(gt), RLX);
        }
        const char* pkv = ws + WS_KV + h * 262144; const char* pq = ws + WS_QF + (h * 64 + 2 * rblk) * 2048;
        unsigned alds = lds0 + lane * 16, vkv = w * 4096 + lane * 16, vq = lane * 16;
        bf16x8 ones; unsigned sidx = (lane & 1) ? 0xeeeeeeeeu : 0x44444444u;
#pragma unroll
        for (int i = 0; i < 8; ++i) ones[i] = ((lane >> 5) == ((lane >> 1) & 1)) ? (short)0x3f80 : (short)0;
        asm volatile("" : "+s"(pkv), "+s"(pq), "+v"(alds), "+v"(vkv), "+v"(vq), "+{v[184:187]}"(ones), "+{v190}"(sidx));
        if (w == 0) wait_flags(ws + WS_FA + (h * 32 + r) * 8, tagv);
        asm volatile("s_waitcnt vmcnt(0)\n\ts_barrier" ::: "memory");
        if (lane == 0) __hip_atomic_store((GAS u64*)(ws + WS_FW) + rblk * 32 + h, tagv, RLX);
        __builtin_amdgcn_s_setprio(PRIO_A2);
        u32x16 qf, z0, z1, z2, z3, z4, sx; f32x16 neg;
    asm volatile(
        "s_mov_b64 s[40:41], %[pkv]\n\t"
        "s_and_b32 s41, s41, 0xffff\n\t"
        "s_mov_b32 s42, 0x40000\n\t"
        "s_mov_b32 s43, 0x20000\n\t"
        "s_mov_b32 m0, %[cM]\n\t"
        "global_load_dwordx4 v[120:123], %[vq], %[pq] offset:0 sc1\n\t"
        "global_load_dwordx4 v[124:127], %[vq], %[pq] offset:1024 sc1\n\t"
        "buffer_load_dwordx4 %[vkv], s[40:43], 0 offen offset:0 sc1 lds\n\t"
        "buffer_load_dwordx4 %[vkv], s[40:43], 0 offen offset:1024 sc1 lds\n\t"
        "buffer_load_dwordx4 %[vkv], s[40:43], 0 offen offset:2048 sc1 lds\n\t"
        "buffer_load_dwordx4 %[vkv], s[40:43], 0 offen offset:3072 sc1 lds\n\t"
        "global_load_dwordx4 v[128:131], %[vq], %[pq] offset:2048 sc1\n\t"
        "global_load_dwordx4 v[132:135], %[vq], %[pq] offset:3072 sc1\n\t"
        "s_add_u32 m0, %[cM], 16384\n\t"
        "s_mov_b32 s44, 16384\n\t"
        "buffer_load_dwordx4 %[vkv], s[40:43], s44 offen offset:0 sc1 lds\n\t"
        "buffer_load_dwordx4 %[vkv], s[40:43], s44 offen offset:1024 sc1 lds\n\t"
        "buffer_load_dwordx4 %[vkv], s[40:43], s44 offen offset:2048 sc1 lds\n\t"
        "buffer_load_dwordx4 %[vkv], s[40:43], s44 offen offset:3072 sc1 lds\n\t"
        "s_add_u32 m0, %[cM], 32768\n\t"
        "s_mov_b32 s44, 32768\n\t"
        "buffer_load_dwordx4 %[vkv], s[40:43], s44 offen offset:0 sc1 lds\n\t"
        "buffer_load_dwordx4 %[vkv], s[40:43], s44 offen offset:1024 sc1 lds\n\t"
        "buffer_load_dwordx4 %[vkv], s[40:43], s44 offen offset:2048 sc1 lds\n\t"
        "buffer_load_dwordx4 %[vkv], s[40:43], s44 offen offset:3072 sc1 lds\n\t"
        "v_mov_b32 v88, 0\n\t"
        "v_mov_b32 v89, 0\n\t"
        "v_mov_b32 v90, 0\n\t"
        "v_mov_b32 v91, 0\n\t"
        "v_mov_b32 v92, 0\n\t"
        "v_mov_b32 v93, 0\n\t"
        "v_mov_b32 v94, 0\n\t"
        "v_mov_b32 v95, 0\n\t"
        "v_mov_b32 v96, 0\n\t"
        "v_mov_b32 v97, 0\n\t"
        "v_mov_b32 v98, 0\n\t"
        "v_mov_b32 v99, 0\n\t"
        "v_mov_b32 v100, 0\n\t"
        "v_mov_b32 v101, 0\n\t"
        "v_mov_b32 v102, 0\n\t"
        "v_mov_b32 v103, 0\n\t"
        "v_mov_b32 v104, 0\n\t"
        "v_mov_b32 v105, 0\n\t"
        "v_mov_b32 v106, 0\n\t"
        "v_mov_b32 v107, 0\n\t"
        "v_mov_b32 v108, 0\n\t"
        "v_mov_b32 v109, 0\n\t"
        "v_mov_b32 v110, 0\n\t"
        "v_mov_b32 v111, 0\n\t"
        "v_mov_b32 v112, 0\n\t"
        "v_mov_b32 v113, 0\n\t"
        "v_mov_b32 v114, 0\n\t"
        "v_mov_b32 v115, 0\n\t"
        "v_mov_b32 v116, 0\n\t"
        "v_mov_b32 v117, 0\n\t"
        "v_mov_b32 v118, 0\n\t"
        "v_mov_b32 v119, 0\n\t"
        "v_mov_b32 v160, 0\n\t"
        "v_mov_b32 v161, 0\n\t"
        "v_mov_b32 v162, 0\n\t"
        "v_mov_b32 v163, 0\n\t"
        "v_mov_b32 v164, 0\n\t"
        "v_mov_b32 v165, 0\n\t"
        "v_mov_b32 v166, 0\n\t"
        "v_mov_b32 v167, 0\n\t"
        "v_mov_b32 v168, 0\n\t"
        "v_mov_b32 v169, 0\n\t"
        "v_mov_b32 v170, 0\n\t"
        "v_mov_b32 v171, 0\n\t"
        "v_mov_b32 v172, 0\n\t"
        "v_mov_b32 v173, 0\n\t"
        "v_mov_b32 v174, 0\n\t"
        "v_mov_b32 v175, 0\n\t"
        "v_mov_b32 v176, 0\n\t"
        "v_mov_b32 v177, 0\n\t"
        "v_mov_b32 v178, 0\n\t"
        "v_mov_b32 v179, 0\n\t"
        "v_mov_b32 v180, 0\n\t"
        "v_mov_b32 v181, 0\n\t"
        "v_mov_b32 v182, 0\n\t"
        "v_mov_b32 v183, 0\n\t"
        "s_waitcnt vmcnt(8)\n\t"
        "s_barrier\n\t"
        "ds_read_b128 v[136:139], %[alds] offset:0\n\t"
        "ds_read_b128 v[140:143], %[alds] offset:1024\n\t"
        "ds_read_b128 v[152:155], %[alds] offset:2048\n\t"
        "ds_read_b128 v[156:159], %[alds] offset:3072\n\t"
        "s_waitcnt lgkmcnt(2)\n\t"
        "v_mfma_f32_16x16x32_bf16 v[56:59], v[136:139], v[120:123], 0\n\t"
        "v_mfma_f32_16x16x32_bf16 v[60:63], v[140:143], v[120:123], 0\n\t"
        "v_mfma_f32_16x16x32_bf16 v[64:67], v[136:139], v[124:127], 0\n\t"
        "v_mfma_f32_16x16x32_bf16 v[68:71], v[140:143], v[124:127], 0\n\t"
        "v_mfma_f32_16x16x32_bf16 v[72:75], v[136:139], v[128:131], 0\n\t"
        "v_mfma_f32_16x16x32_bf16 v[76:79], v[140:143], v[128:131], 0\n\t"
        "v_mfma_f32_16x16x32_bf16 v[80:83], v[136:139], v[132:135], 0\n\t"
        "v_mfma_f32_16x16x32_bf16 v[84:87], v[140:143], v[132:135], 0\n\t"
        "s_nop 1\n\t"
        "v_max3_f32 v40, v56, v57, v58\n\t"
        "v_max3_f32 v41, v59, v60, v61\n\t"
        "v_max3_f32 v40, v40, v62, v63\n\t"
        "v_max_f32 v40, v40, v41\n\t"
        "v_max3_f32 v44, v64, v65, v66\n\t"
        "v_max3_f32 v45, v67, v68, v69\n\t"
        "v_max3_f32 v44, v44, v70, v71\n\t"
        "v_max_f32 v44, v44, v45\n\t"
        "v_max3_f32 v48, v72, v73, v74\n\t"
        "v_max3_f32 v49, v75, v76, v77\n\t"
        "v_max3_f32 v48, v48, v78, v79\n\t"
        "v_max_f32 v48, v48, v49\n\t"
        "v_max3_f32 v52, v80, v81, v82\n\t"
        "v_max3_f32 v53, v83, v84, v85\n\t"
        "v_max3_f32 v52, v52, v86, v87\n\t"
        "v_max_f32 v52, v52, v53\n\t"
        "v_mov_b32 v41, v40\n\t"
        "v_mov_b32 v45, v44\n\t"
        "v_mov_b32 v49, v48\n\t"
        "v_mov_b32 v53, v52\n\t"
        "s_nop 0\n\t"
        "v_permlane16_swap_b32 v40, v41\n\t"
        "v_permlane16_swap_b32 v44, v45\n\t"
        "v_permlane16_swap_b32 v48, v49\n\t"
        "v_permlane16_swap_b32 v52, v53\n\t"
        "v_max_f32 v40, v40, v41\n\t"
        "v_max_f32 v44, v44, v45\n\t"
        "v_max_f32 v48, v48, v49\n\t"
        "v_max_f32 v52, v52, v53\n\t"
        "v_mov_b32 v41, v40\n\t"
        "v_mov_b32 v45, v44\n\t"
        "v_mov_b32 v49, v48\n\t"
        "v_mov_b32 v53, v52\n\t"
        "s_nop 0\n\t"
        "v_permlane32_swap_b32 v40, v41\n\t"
        "v_permlane32_swap_b32 v44, v45\n\t"
        "v_permlane32_swap_b32 v48, v49\n\t"
        "v_permlane32_swap_b32 v52, v53\n\t"
        "v_min_f32 v40, -v40, -v41\n\t"
        "v_min_f32 v44, -v44, -v45\n\t"
        "v_min_f32 v48, -v48, -v49\n\t"
        "v_min_f32 v52, -v52, -v53\n\t"
        "v_mov_b32 v41, v40\n\t"
        "v_mov_b32 v42, v40\n\t"
        "v_mov_b32 v43, v40\n\t"
        "v_mov_b32 v45, v44\n\t"
        "v_mov_b32 v46, v44\n\t"
        "v_mov_b32 v47, v44\n\t"
        "v_mov_b32 v49, v48\n\t"
        "v_mov_b32 v50, v48\n\t"
        "v_mov_b32 v51, v48\n\t"
        "v_mov_b32 v53, v52\n\t"
        "v_mov_b32 v54, v52\n\t"
        "v_mov_b32 v55, v52\n\t"
        "v_add_f32 v56, v56, v40\n\t"
        "v_add_f32 v57, v57, v40\n\t"
        "v_add_f32 v58, v58, v40\n\t"
        "v_add_f32 v59, v59, v40\n\t"
        "v_add_f32 v60, v60, v40\n\t"
        "v_add_f32 v61, v61, v40\n\t"
        "v_add_f32 v62, v62, v40\n\t"
        "v_add_f32 v63, v63, v40\n\t"
        "v_add_f32 v64, v64, v44\n\t"
        "v_add_f32 v65, v65, v44\n\t"
        "v_add_f32 v66, v66, v44\n\t"
        "v_add_f32 v67, v67, v44\n\t"
        "v_add_f32 v68, v68, v44\n\t"
        "v_add_f32 v69, v69, v44\n\t"
        "v_add_f32 v70, v70, v44\n\t"
        "v_add_f32 v71, v71, v44\n\t"
        "s_waitcnt lgkmcnt(0)\n\t"
        : "=&{v[120:135]}"(qf), "=&{v[88:103]}"(z0), "=&{v[104:119]}"(z1), "=&{v[136:151]}"(z2), "=&{v[152:167]}"(z3), "=&{v[168:183]}"(z4), "=&{v[40:55]}"(neg), "=&{v[56:71]}"(sx)
        : [vq]"v"(vq), [pq]"s"(pq), [alds]"v"(alds), [vkv]"v"(vkv), [cM]"s"(cM), [pkv]"s"(pkv)
        : "v72", "v73", "v74", "v75", "v76", "v77", "v78", "v79", "v80", "v81", "v82", "v83", "v84", "v85", "v86", "v87", "s40", "s41", "s42", "s43", "s44", "scc", "memory");

        f32x8 chk;
    asm volatile(
        "s_mov_b64 s[40:41], %[pkv]\n\t"
        "s_and_b32 s41, s41, 0xffff\n\t"
        "s_mov_b32 s42, 0x40000\n\t"
        "s_mov_b32 s43, 0x20000\n\t"
        "s_mov_b32 s44, 49152\n\t"
        "s_mov_b32 s45, 16384\n\t"
        "s_mov_b32 s46, 0\n\t"
        "v_mov_b32 v188, %[alds]\n\t"
        "v_add_u32 v189, 16384, %[alds]\n\t"
        "1:\n\t"
        "v_mfma_f32_16x16x32_bf16 v[72:75], v[136:139], v[128:131], v[48:51]\n\t"
        "v_exp_f32 v56, v56\n\t"
        "v_mfma_f32_16x16x32_bf16 v[76:79], v[140:143], v[128:131], v[48:51]\n\t"
        "v_exp_f32 v57, v57\n\t"
        "v_mfma_f32_16x16x32_bf16 v[112:115], v[160:163], v[172:175], v[112:115]\n\t"
        "v_exp_f32 v58, v58\n\t"
        "v_mfma_f32_16x16x32_bf16 v[116:119], v[164:167], v[172:175], v[116:119]\n\t"
        "v_exp_f32 v59, v59\n\t"
        "v_smfmac_f32_16x16x64_bf16 v[180:183], v[184:187], v[168:175], v190\n\t"
        "ds_read_b128 v[144:147], v188 offset:4096\n\t"
        "v_exp_f32 v60, v60\n\t"
        "v_exp_f32 v61, v61\n\t"
        "ds_read_b128 v[148:151], v188 offset:5120\n\t"
        "v_exp_f32 v62, v62\n\t"
        "v_exp_f32 v63, v63\n\t"
        "v_cvt_pk_bf16_f32 v168, v56, v57\n\t"
        "v_cvt_pk_bf16_f32 v169, v58, v59\n\t"
        "ds_read_b128 v[160:163], v188 offset:6144\n\t"
        "v_cvt_pk_bf16_f32 v170, v60, v61\n\t"
        "v_cvt_pk_bf16_f32 v171, v62, v63\n\t"
        "v_exp_f32 v64, v64\n\t"
        "v_mfma_f32_16x16x32_bf16 v[80:83], v[136:139], v[132:135], v[52:55]\n\t"
        "v_exp_f32 v65, v65\n\t"
        "v_mfma_f32_16x16x32_bf16 v[84:87], v[140:143], v[132:135], v[52:55]\n\t"
        "v_exp_f32 v66, v66\n\t"
        "v_mfma_f32_16x16x32_bf16 v[88:91], v[152:155], v[168:171], v[88:91]\n\t"
        "v_exp_f32 v67, v67\n\t"
        "v_mfma_f32_16x16x32_bf16 v[92:95], v[156:159], v[168:171], v[92:95]\n\t"
        "ds_read_b128 v[164:167], v188 offset:7168\n\t"
        "v_exp_f32 v68, v68\n\t"
        "v_exp_f32 v69, v69\n\t"
        "v_exp_f32 v70, v70\n\t"
        "v_exp_f32 v71, v71\n\t"
        "v_cvt_pk_bf16_f32 v172, v64, v65\n\t"
        "v_cvt_pk_bf16_f32 v173, v66, v67\n\t"
        "v_cvt_pk_bf16_f32 v174, v68, v69\n\t"
        "v_cvt_pk_bf16_f32 v175, v70, v71\n\t"
        "s_waitcnt lgkmcnt(2)\n\t"
        "v_mfma_f32_16x16x32_bf16 v[56:59], v[144:147], v[120:123], v[40:43]\n\t"
        "v_exp_f32 v72, v72\n\t"
        "v_mfma_f32_16x16x32_bf16 v[60:63], v[148:151], v[120:123], v[40:43]\n\t"
        "v_exp_f32 v73, v73\n\t"
        "v_mfma_f32_16x16x32_bf16 v[96:99], v[152:155], v[172:175], v[96:99]\n\t"
        "v_exp_f32 v74, v74\n\t"
        "v_mfma_f32_16x16x32_bf16 v[100:103], v[156:159], v[172:175], v[100:103]\n\t"
        "v_exp_f32 v75, v75\n\t"
        "v_smfmac_f32_16x16x64_bf16 v[176:179], v[184:187], v[168:175], v190\n\t"
        "v_exp_f32 v76, v76\n\t"
        "v_exp_f32 v77, v77\n\t"
        "v_exp_f32 v78, v78\n\t"
        "v_exp_f32 v79, v79\n\t"
        "v_cvt_pk_bf16_f32 v168, v72, v73\n\t"
        "v_cvt_pk_bf16_f32 v169, v74, v75\n\t"
        "v_cvt_pk_bf16_f32 v170, v76, v77\n\t"
        "v_cvt_pk_bf16_f32 v171, v78, v79\n\t"
        "v_exp_f32 v80, v80\n\t"
        "v_mfma_f32_16x16x32_bf16 v[64:67], v[144:147], v[124:127], v[44:47]\n\t"
        "v_exp_f32 v81, v81\n\t"
        "v_mfma_f32_16x16x32_bf16 v[68:71], v[148:151], v[124:127], v[44:47]\n\t"
        "v_exp_f32 v82, v82\n\t"
        "v_mfma_f32_16x16x32_bf16 v[104:107], v[152:155], v[168:171], v[104:107]\n\t"
        "v_exp_f32 v83, v83\n\t"
        "v_mfma_f32_16x16x32_bf16 v[108:111], v[156:159], v[168:171], v[108:111]\n\t"
        "v_exp_f32 v84, v84\n\t"
        "v_exp_f32 v85, v85\n\t"
        "s_waitcnt lgkmcnt(0)\n\t"
        "v_exp_f32 v86, v86\n\t"
        "v_exp_f32 v87, v87\n\t"
        "v_cvt_pk_bf16_f32 v172, v80, v81\n\t"
        "v_cvt_pk_bf16_f32 v173, v82, v83\n\t"
        "v_cvt_pk_bf16_f32 v174, v84, v85\n\t"
        "v_cvt_pk_bf16_f32 v175, v86, v87\n\t"
        "v_mfma_f32_16x16x32_bf16 v[72:75], v[144:147], v[128:131], v[48:51]\n\t"
        "v_exp_f32 v56, v56\n\t"
        "v_mfma_f32_16x16x32_bf16 v[76:79], v[148:151], v[128:131], v[48:51]\n\t"
        "v_exp_f32 v57, v57\n\t"
        "v_mfma_f32_16x16x32_bf16 v[112:115], v[152:155], v[172:175], v[112:115]\n\t"
        "v_exp_f32 v58, v58\n\t"
        "v_mfma_f32_16x16x32_bf16 v[116:119], v[156:159], v[172:175], v[116:119]\n\t"
        "v_exp_f32 v59, v59\n\t"
        "v_smfmac_f32_16x16x64_bf16 v[180:183], v[184:187], v[168:175], v190\n\t"
        "ds_read_b128 v[136:139], v188 offset:8192\n\t"
        "v_exp_f32 v60, v60\n\t"
        "v_exp_f32 v61, v61\n\t"
        "ds_read_b128 v[140:143], v188 offset:9216\n\t"
        "v_exp_f32 v62, v62\n\t"
        "v_exp_f32 v63, v63\n\t"
        "v_cvt_pk_bf16_f32 v168, v56, v57\n\t"
        "v_cvt_pk_bf16_f32 v169, v58, v59\n\t"
        "ds_read_b128 v[152:155], v188 offset:10240\n\t"
        "v_cvt_pk_bf16_f32 v170, v60, v61\n\t"
        "v_cvt_pk_bf16_f32 v171, v62, v63\n\t"
        "v_exp_f32 v64, v64\n\t"
        "v_mfma_f32_16x16x32_bf16 v[80:83], v[144:147], v[132:135], v[52:55]\n\t"
        "v_exp_f32 v65, v65\n\t"
        "v_mfma_f32_16x16x32_bf16 v[84:87], v[148:151], v[132:135], v[52:55]\n\t"
        "v_exp_f32 v66, v66\n\t"
        "v_mfma_f32_16x16x32_bf16 v[88:91], v[160:163], v[168:171], v[88:91]\n\t"
        "v_exp_f32 v67, v67\n\t"
        "v_mfma_f32_16x16x32_bf16 v[92:95], v[164:167], v[168:171], v[92:95]\n\t"
        "ds_read_b128 v[156:159], v188 offset:11264\n\t"
        "v_exp_f32 v68, v68\n\t"
        "v_exp_f32 v69, v69\n\t"
        "v_exp_f32 v70, v70\n\t"
        "v_exp_f32 v71, v71\n\t"
        "v_cvt_pk_bf16_f32 v172, v64, v65\n\t"
        "v_cvt_pk_bf16_f32 v173, v66, v67\n\t"
        "v_cvt_pk_bf16_f32 v174, v68, v69\n\t"
        "v_cvt_pk_bf16_f32 v175, v70, v71\n\t"
        "s_waitcnt lgkmcnt(2)\n\t"
        "v_mfma_f32_16x16x32_bf16 v[56:59], v[136:139], v[120:123], v[40:43]\n\t"
        "v_exp_f32 v72, v72\n\t"
        "v_mfma_f32_16x16x32_bf16 v[60:63], v[140:143], v[120:123], v[40:43]\n\t"
        "v_exp_f32 v73, v73\n\t"
        "v_mfma_f32_16x16x32_bf16 v[96:99], v[160:163], v[172:175], v[96:99]\n\t"
        "v_exp_f32 v74, v74\n\t"
        "v_mfma_f32_16x16x32_bf16 v[100:103], v[164:167], v[172:175], v[100:103]\n\t"
        "v_exp_f32 v75, v75\n\t"
        "v_smfmac_f32_16x16x64_bf16 v[176:179], v[184:187], v[168:175], v190\n\t"
        "v_exp_f32 v76, v76\n\t"
        "v_exp_f32 v77, v77\n\t"
        "v_exp_f32 v78, v78\n\t"
        "v_exp_f32 v79, v79\n\t"
        "v_cvt_pk_bf16_f32 v168, v72, v73\n\t"
        "v_cvt_pk_bf16_f32 v169, v74, v75\n\t"
        "v_cvt_pk_bf16_f32 v170, v76, v77\n\t"
        "v_cvt_pk_bf16_f32 v171, v78, v79\n\t"
        "v_exp_f32 v80, v80\n\t"
        "v_mfma_f32_16x16x32_bf16 v[64:67], v[136:139], v[124:127], v[44:47]\n\t"
        "v_exp_f32 v81, v81\n\t"
        "v_mfma_f32_16x16x32_bf16 v[68:71], v[140:143], v[124:127], v[44:47]\n\t"
        "v_exp_f32 v82, v82\n\t"
        "v_mfma_f32_16x16x32_bf16 v[104:107], v[160:163], v[168:171], v[104:107]\n\t"
        "v_exp_f32 v83, v83\n\t"
        "v_mfma_f32_16x16x32_bf16 v[108:111], v[164:167], v[168:171], v[108:111]\n\t"
        "v_exp_f32 v84, v84\n\t"
        "v_exp_f32 v85, v85\n\t"
        "s_waitcnt lgkmcnt(0)\n\t"
        "v_exp_f32 v86, v86\n\t"
        "v_exp_f32 v87, v87\n\t"
        "v_cvt_pk_bf16_f32 v172, v80, v81\n\t"
        "v_cvt_pk_bf16_f32 v173, v82, v83\n\t"
        "v_cvt_pk_bf16_f32 v174, v84, v85\n\t"
        "v_cvt_pk_bf16_f32 v175, v86, v87\n\t"
        "v_mfma_f32_16x16x32_bf16 v[72:75], v[136:139], v[128:131], v[48:51]\n\t"
        "v_exp_f32 v56, v56\n\t"
        "v_mfma_f32_16x16x32_bf16 v[76:79], v[140:143], v[128:131], v[48:51]\n\t"
        "v_exp_f32 v57, v57\n\t"
        "v_mfma_f32_16x16x32_bf16 v[112:115], v[160:163], v[172:175], v[112:115]\n\t"
        "v_exp_f32 v58, v58\n\t"
        "v_mfma_f32_16x16x32_bf16 v[116:119], v[164:167], v[172:175], v[116:119]\n\t"
        "v_exp_f32 v59, v59\n\t"
        "v_smfmac_f32_16x16x64_bf16 v[180:183], v[184:187], v[168:175], v190\n\t"
        "ds_read_b128 v[144:147], v188 offset:12288\n\t"
        "v_exp_f32 v60, v60\n\t"
        "v_exp_f32 v61, v61\n\t"
        "ds_read_b128 v[148:151], v188 offset:13312\n\t"
        "v_exp_f32 v62, v62\n\t"
        "v_exp_f32 v63, v63\n\t"
        "v_cvt_pk_bf16_f32 v168, v56, v57\n\t"
        "v_cvt_pk_bf16_f32 v169, v58, v59\n\t"
        "ds_read_b128 v[160:163], v188 offset:14336\n\t"
        "v_cvt_pk_bf16_f32 v170, v60, v61\n\t"
        "v_cvt_pk_bf16_f32 v171, v62, v63\n\t"
        "v_exp_f32 v64, v64\n\t"
        "v_mfma_f32_16x16x32_bf16 v[80:83], v[136:139], v[132:135], v[52:55]\n\t"
        "v_exp_f32 v65, v65\n\t"
        "v_mfma_f32_16x16x32_bf16 v[84:87], v[140:143], v[132:135], v[52:55]\n\t"
        "v_exp_f32 v66, v66\n\t"
        "v_mfma_f32_16x16x32_bf16 v[88:91], v[152:155], v[168:171], v[88:91]\n\t"
        "v_exp_f32 v67, v67\n\t"
        "v_mfma_f32_16x16x32_bf16 v[92:95], v[156:159], v[168:171], v[92:95]\n\t"
        "ds_read_b128 v[164:167], v188 offset:15360\n\t"
        "v_exp_f32 v68, v68\n\t"
        "v_exp_f32 v69, v69\n\t"
        "v_exp_f32 v70, v70\n\t"
        "v_exp_f32 v71, v71\n\t"
        "v_cvt_pk_bf16_f32 v172, v64, v65\n\t"
        "v_cvt_pk_bf16_f32 v173, v66, v67\n\t"
        "v_cvt_pk_bf16_f32 v174, v68, v69\n\t"
        "v_cvt_pk_bf16_f32 v175, v70, v71\n\t"
        "s_waitcnt lgkmcnt(2)\n\t"
        "v_mfma_f32_16x16x32_bf16 v[56:59], v[144:147], v[120:123], v[40:43]\n\t"
        "v_exp_f32 v72, v72\n\t"
        "v_mfma_f32_16x16x32_bf16 v[60:63], v[148:151], v[120:123], v[40:43]\n\t"
        "v_exp_f32 v73, v73\n\t"
        "v_mfma_f32_16x16x32_bf16 v[96:99], v[152:155], v[172:175], v[96:99]\n\t"
        "v_exp_f32 v74, v74\n\t"
        "v_mfma_f32_16x16x32_bf16 v[100:103], v[156:159], v[172:175], v[100:103]\n\t"
        "v_exp_f32 v75, v75\n\t"
        "v_smfmac_f32_16x16x64_bf16 v[176:179], v[184:187], v[168:175], v190\n\t"
        "v_exp_f32 v76, v76\n\t"
        "v_exp_f32 v77, v77\n\t"
        "v_exp_f32 v78, v78\n\t"
        "v_exp_f32 v79, v79\n\t"
        "v_cvt_pk_bf16_f32 v168, v72, v73\n\t"
        "v_cvt_pk_bf16_f32 v169, v74, v75\n\t"
        "v_cvt_pk_bf16_f32 v170, v76, v77\n\t"
        "v_cvt_pk_bf16_f32 v171, v78, v79\n\t"
        "v_exp_f32 v80, v80\n\t"
        "v_mfma_f32_16x16x32_bf16 v[64:67], v[144:147], v[124:127], v[44:47]\n\t"
        "v_exp_f32 v81, v81\n\t"
        "v_mfma_f32_16x16x32_bf16 v[68:71], v[148:151], v[124:127], v[44:47]\n\t"
        "v_exp_f32 v82, v82\n\t"
        "v_mfma_f32_16x16x32_bf16 v[104:107], v[152:155], v[168:171], v[104:107]\n\t"
        "v_exp_f32 v83, v83\n\t"
        "v_mfma_f32_16x16x32_bf16 v[108:111], v[156:159], v[168:171], v[108:111]\n\t"
        "v_exp_f32 v84, v84\n\t"
        "v_exp_f32 v85, v85\n\t"
        "s_waitcnt lgkmcnt(0)\n\t"
        "v_exp_f32 v86, v86\n\t"
        "v_exp_f32 v87, v87\n\t"
        "v_cvt_pk_bf16_f32 v172, v80, v81\n\t"
        "v_cvt_pk_bf16_f32 v173, v82, v83\n\t"
        "v_cvt_pk_bf16_f32 v174, v84, v85\n\t"
        "v_cvt_pk_bf16_f32 v175, v86, v87\n\t"
        "s_add_u32 s47, s45, 32768\n\t"
        "s_and_b32 s47, s47, 0xffff\n\t"
        "s_waitcnt vmcnt(4)\n\t"
        "s_barrier\n\t"
        "v_mfma_f32_16x16x32_bf16 v[72:75], v[144:147], v[128:131], v[48:51]\n\t"
        "s_add_u32 m0, s47, %[cM]\n\t"
        "v_exp_f32 v56, v56\n\t"
        "v_mfma_f32_16x16x32_bf16 v[76:79], v[148:151], v[128:131], v[48:51]\n\t"
        "v_exp_f32 v57, v57\n\t"
        "v_mfma_f32_16x16x32_bf16 v[112:115], v[152:155], v[172:175], v[112:115]\n\t"
        "v_exp_f32 v58, v58\n\t"
        "v_mfma_f32_16x16x32_bf16 v[116:119], v[156:159], v[172:175], v[116:119]\n\t"
        "v_exp_f32 v59, v59\n\t"
        "v_smfmac_f32_16x16x64_bf16 v[180:183], v[184:187], v[168:175], v190\n\t"
        "ds_read_b128 v[136:139], v189 offset:0\n\t"
        "v_exp_f32 v60, v60\n\t"
        "v_exp_f32 v61, v61\n\t"
        "ds_read_b128 v[140:143], v189 offset:1024\n\t"
        "v_exp_f32 v62, v62\n\t"
        "v_exp_f32 v63, v63\n\t"
        "v_cvt_pk_bf16_f32 v168, v56, v57\n\t"
        "v_cvt_pk_bf16_f32 v169, v58, v59\n\t"
        "ds_read_b128 v[152:155], v189 offset:2048\n\t"
        "v_cvt_pk_bf16_f32 v170, v60, v61\n\t"
        "v_cvt_pk_bf16_f32 v171, v62, v63\n\t"
        "v_exp_f32 v64, v64\n\t"
        "v_mfma_f32_16x16x32_bf16 v[80:83], v[144:147], v[132:135], v[52:55]\n\t"
        "v_exp_f32 v65, v65\n\t"
        "v_mfma_f32_16x16x32_bf16 v[84:87], v[148:151], v[132:135], v[52:55]\n\t"
        "v_exp_f32 v66, v66\n\t"
        "v_mfma_f32_16x16x32_bf16 v[88:91], v[160:163], v[168:171], v[88:91]\n\t"
        "v_exp_f32 v67, v67\n\t"
        "v_mfma_f32_16x16x32_bf16 v[92:95], v[164:167], v[168:171], v[92:95]\n\t"
        "ds_read_b128 v[156:159], v189 offset:3072\n\t"
        "v_exp_f32 v68, v68\n\t"
        "v_exp_f32 v69, v69\n\t"
        "buffer_load_dwordx4 %[vkv], s[40:43], s44 offen offset:0 sc1 lds\n\t"
        "v_exp_f32 v70, v70\n\t"
        "v_exp_f32 v71, v71\n\t"
        "v_cvt_pk_bf16_f32 v172, v64, v65\n\t"
        "v_cvt_pk_bf16_f32 v173, v66, v67\n\t"
        "v_cvt_pk_bf16_f32 v174, v68, v69\n\t"
        "v_cvt_pk_bf16_f32 v175, v70, v71\n\t"
        "s_waitcnt lgkmcnt(2)\n\t"
        "v_mfma_f32_16x16x32_bf16 v[56:59], v[136:139], v[120:123], v[40:43]\n\t"
        "v_exp_f32 v72, v72\n\t"
        "v_mfma_f32_16x16x32_bf16 v[60:63], v[140:143], v[120:123], v[40:43]\n\t"
        "v_exp_f32 v73, v73\n\t"
        "v_mfma_f32_16x16x32_bf16 v[96:99], v[160:163], v[172:175], v[96:99]\n\t"
        "v_exp_f32 v74, v74\n\t"
        "v_mfma_f32_16x16x32_bf16 v[100:103], v[164:167], v[172:175], v[100:103]\n\t"
        "v_exp_f32 v75, v75\n\t"
        "v_smfmac_f32_16x16x64_bf16 v[176:179], v[184:187], v[168:175], v190\n\t"
        "buffer_load_dwordx4 %[vkv], s[40:43], s44 offen offset:1024 sc1 lds\n\t"
        "v_exp_f32 v76, v76\n\t"
        "v_exp_f32 v77, v77\n\t"
        "v_exp_f32 v78, v78\n\t"
        "v_exp_f32 v79, v79\n\t"
        "v_cvt_pk_bf16_f32 v168, v72, v73\n\t"
        "v_cvt_pk_bf16_f32 v169, v74, v75\n\t"
        "buffer_load_dwordx4 %[vkv], s[40:43], s44 offen offset:2048 sc1 lds\n\t"
        "v_cvt_pk_bf16_f32 v170, v76, v77\n\t"
        "v_cvt_pk_bf16_f32 v171, v78, v79\n\t"
        "v_exp_f32 v80, v80\n\t"
        "v_mfma_f32_16x16x32_bf16 v[64:67], v[136:139], v[124:127], v[44:47]\n\t"
        "v_exp_f32 v81, v81\n\t"
        "v_mfma_f32_16x16x32_bf16 v[68:71], v[140:143], v[124:127], v[44:47]\n\t"
        "v_exp_f32 v82, v82\n\t"
        "v_mfma_f32_16x16x32_bf16 v[104:107], v[160:163], v[168:171], v[104:107]\n\t"
        "v_exp_f32 v83, v83\n\t"
        "v_mfma_f32_16x16x32_bf16 v[108:111], v[164:167], v[168:171], v[108:111]\n\t"
        "buffer_load_dwordx4 %[vkv], s[40:43], s44 offen offset:3072 sc1 lds\n\t"
        "v_exp_f32 v84, v84\n\t"
        "v_exp_f32 v85, v85\n\t"
        "s_waitcnt lgkmcnt(0)\n\t"
        "v_exp_f32 v86, v86\n\t"
        "v_exp_f32 v87, v87\n\t"
        "v_cvt_pk_bf16_f32 v172, v80, v81\n\t"
        "v_cvt_pk_bf16_f32 v173, v82, v83\n\t"
        "v_cvt_pk_bf16_f32 v174, v84, v85\n\t"
        "v_cvt_pk_bf16_f32 v175, v86, v87\n\t"
        "s_add_u32 s45, s45, 16384\n\t"
        "s_and_b32 s45, s45, 0xffff\n\t"
        "s_add_u32 s44, s44, 16384\n\t"
        "v_mov_b32 v188, v189\n\t"
        "v_add_u32 v189, s45, %[alds]\n\t"
        "s_add_u32 s46, s46, 1\n\t"
        "s_cmp_lt_u32 s46, 16\n\t"
        "s_cbranch_scc1 1b\n\t"
        "v_mfma_f32_16x16x32_bf16 v[112:115], v[160:163], v[172:175], v[112:115]\n\t"
        "v_mfma_f32_16x16x32_bf16 v[116:119], v[164:167], v[172:175], v[116:119]\n\t"
        "v_smfmac_f32_16x16x64_bf16 v[180:183], v[184:187], v[168:175], v190\n\t"
        "v_add_f32 v72, v176, v177\n\t"
        "v_mul_f32 v76, 0, v88\n\t"
        "v_rcp_f32 v80, v72\n\t"
        "v_fma_f32 v76, v89, 0, v76\n\t"
        "v_fma_f32 v76, v90, 0, v76\n\t"
        "v_fma_f32 v76, v91, 0, v76\n\t"
        "v_fma_f32 v76, v92, 0, v76\n\t"
        "v_fma_f32 v76, v93, 0, v76\n\t"
        "v_fma_f32 v76, v94, 0, v76\n\t"
        "v_fma_f32 v76, v95, 0, v76\n\t"
        "v_mul_f32 v88, v88, v80\n\t"
        "v_mul_f32 v89, v89, v80\n\t"
        "v_mul_f32 v90, v90, v80\n\t"
        "v_mul_f32 v91, v91, v80\n\t"
        "v_mul_f32 v92, v92, v80\n\t"
        "v_mul_f32 v93, v93, v80\n\t"
        "v_mul_f32 v94, v94, v80\n\t"
        "v_mul_f32 v95, v95, v80\n\t"
        "v_cvt_pk_bf16_f32 v56, v88, v89\n\t"
        "v_cvt_pk_bf16_f32 v57, v90, v91\n\t"
        "v_cvt_pk_bf16_f32 v58, v92, v93\n\t"
        "v_cvt_pk_bf16_f32 v59, v94, v95\n\t"
        "v_add_f32 v73, v178, v179\n\t"
        "v_fma_f32 v76, v96, 0, v76\n\t"
        "v_rcp_f32 v81, v73\n\t"
        "v_fma_f32 v76, v97, 0, v76\n\t"
        "v_fma_f32 v76, v98, 0, v76\n\t"
        "v_fma_f32 v76, v99, 0, v76\n\t"
        "v_fma_f32 v76, v100, 0, v76\n\t"
        "v_fma_f32 v76, v101, 0, v76\n\t"
        "v_fma_f32 v76, v102, 0, v76\n\t"
        "v_fma_f32 v76, v103, 0, v76\n\t"
        "v_mul_f32 v96, v96, v81\n\t"
        "v_mul_f32 v97, v97, v81\n\t"
        "v_mul_f32 v98, v98, v81\n\t"
        "v_mul_f32 v99, v99, v81\n\t"
        "v_mul_f32 v100, v100, v81\n\t"
        "v_mul_f32 v101, v101, v81\n\t"
        "v_mul_f32 v102, v102, v81\n\t"
        "v_mul_f32 v103, v103, v81\n\t"
        "v_cvt_pk_bf16_f32 v60, v96, v97\n\t"
        "v_cvt_pk_bf16_f32 v61, v98, v99\n\t"
        "v_cvt_pk_bf16_f32 v62, v100, v101\n\t"
        "v_cvt_pk_bf16_f32 v63, v102, v103\n\t"
        "v_add_f32 v74, v180, v181\n\t"
        "v_fma_f32 v76, v104, 0, v76\n\t"
        "v_rcp_f32 v82, v74\n\t"
        "v_fma_f32 v76, v105, 0, v76\n\t"
        "v_fma_f32 v76, v106, 0, v76\n\t"
        "v_fma_f32 v76, v107, 0, v76\n\t"
        "v_fma_f32 v76, v108, 0, v76\n\t"
        "v_fma_f32 v76, v109, 0, v76\n\t"
        "v_fma_f32 v76, v110, 0, v76\n\t"
        "v_fma_f32 v76, v111, 0, v76\n\t"
        "v_mul_f32 v104, v104, v82\n\t"
        "v_mul_f32 v105, v105, v82\n\t"
        "v_mul_f32 v106, v106, v82\n\t"
        "v_mul_f32 v107, v107, v82\n\t"
        "v_mul_f32 v108, v108, v82\n\t"
        "v_mul_f32 v109, v109, v82\n\t"
        "v_mul_f32 v110, v110, v82\n\t"
        "v_mul_f32 v111, v111, v82\n\t"
        "v_cvt_pk_bf16_f32 v64, v104, v105\n\t"
        "v_cvt_pk_bf16_f32 v65, v106, v107\n\t"
        "v_cvt_pk_bf16_f32 v66, v108, v109\n\t"
        "v_cvt_pk_bf16_f32 v67, v110, v111\n\t"
        "v_add_f32 v75, v182, v183\n\t"
        "v_fma_f32 v76, v112, 0, v76\n\t"
        "v_rcp_f32 v83, v75\n\t"
        "v_fma_f32 v76, v113, 0, v76\n\t"
        "v_fma_f32 v76, v114, 0, v76\n\t"
        "v_fma_f32 v76, v115, 0, v76\n\t"
        "v_fma_f32 v76, v116, 0, v76\n\t"
        "v_fma_f32 v76, v117, 0, v76\n\t"
        "v_fma_f32 v76, v118, 0, v76\n\t"
        "v_fma_f32 v76, v119, 0, v76\n\t"
        "v_mul_f32 v112, v112, v83\n\t"
        "v_mul_f32 v113, v113, v83\n\t"
        "v_mul_f32 v114, v114, v83\n\t"
        "v_mul_f32 v115, v115, v83\n\t"
        "v_mul_f32 v116, v116, v83\n\t"
        "v_mul_f32 v117, v117, v83\n\t"
        "v_mul_f32 v118, v118, v83\n\t"
        "v_mul_f32 v119, v119, v83\n\t"
        "v_cvt_pk_bf16_f32 v68, v112, v113\n\t"
        "v_cvt_pk_bf16_f32 v69, v114, v115\n\t"
        "v_cvt_pk_bf16_f32 v70, v116, v117\n\t"
        "v_cvt_pk_bf16_f32 v71, v118, v119\n\t"
        : "=&{v[72:79]}"(chk), "+{v[88:103]}"(z0), "+{v[104:119]}"(z1), "+{v[136:151]}"(z2), "+{v[152:167]}"(z3), "+{v[168:183]}"(z4), "+{v[56:71]}"(sx)
        : "{v[40:55]}"(neg), "{v[120:135]}"(qf), "{v[184:187]}"(ones), "{v190}"(sidx), [alds]"v"(alds), [vkv]"v"(vkv), [cM]"s"(cM), [pkv]"s"(pkv)
        : "v80", "v81", "v82", "v83", "v84", "v85", "v86", "v87", "v188", "v189", "s40", "s41", "s42", "s43", "s44", "s45", "s46", "s47", "scc", "memory");
        const unsigned oo = WS_O + rblk * 131072 + (h >> 2) * 16384 + (h & 3) * 1024 + lane * 16;
        if (__builtin_expect(__any(!(chk[0] < 0x1p80f) || !(chk[1] < 0x1p80f) || !(chk[2] < 0x1p80f) || !(chk[3] < 0x1p80f) || !(chk[4] == 0.f)), 0)) {
#pragma unroll 1
            for (int j = 0; j < 4; ++j) __builtin_amdgcn_raw_buffer_store_b128(attn_slow(rs, WS_KV + h * 262144 + lane * 16,
                __builtin_bit_cast(bf16x8, __builtin_amdgcn_raw_buffer_load_b128(rs, WS_QF + (h * 64 + 2 * rblk) * 2048 + j * 1024 + lane * 16, 0, 16))), rs, oo + j * 4096, 0, WT);
        } else {
#pragma unroll
            for (int j = 0; j < 4; ++j) { const u32x4 t = {sx[4 * j], sx[4 * j + 1], sx[4 * j + 2], sx[4 * j + 3]}; __builtin_amdgcn_raw_buffer_store_b128(t, rs, oo + j * 4096, 0, WT); } }
        asm volatile("s_waitcnt vmcnt(0)" ::: "memory");
        if (lane == 0) __hip_atomic_store((GAS u64*)(ws + WS_FO) + rblk * 32 + h, tagv, RLX);
        if (blockIdx.x == 0) {
#pragma unroll 1
            for (int i = 0; i < 8; ++i) wait_flags(ws + WS_FA + (i * 256 + threadIdx.x) * 8, tagv);
            __syncthreads();
            if (threadIdx.x == 0) __hip_atomic_store((GAS u64*)(ws + WS_EP), tagv, RLX);
        }
    }
}

extern "C" void kernel_launch(void* const* d_in, const int* in_sizes, int n_in, void* d_out, int out_size, void* d_ws, size_t ws_size, hipStream_t stream) {
    (void)in_sizes; (void)n_in; (void)out_size; (void)ws_size;
    (void)hipFuncSetAttribute((const void*)k_all, hipFuncAttributeMaxDynamicSharedMemorySize, LDS_BYTES);
    k_all<<<512, 256, LDS_BYTES, stream>>>((const u16*)d_in[0], (const u16*)d_in[1], (const u16*)d_in[2], (const u16*)d_in[3], (const u16*)d_in[4],
                                           (const u16*)d_in[6], (const u16*)d_in[7], (const u16*)d_in[8], (char*)d_ws, (u16*)d_out);
}
```
